```python
import torch

def _softmax(x, axis=-1):
    return torch.softmax(x, dim=axis)
def _gelu(x):
    return torch.nn.functional.gelu(x, approximate="tanh")
def _silu(x):
    return torch.nn.functional.silu(x)
def _sigmoid(x):
    return torch.sigmoid(x)
def _layernorm(x, axis=-1, eps=1e-5):
    m = x.mean(dim=axis, keepdim=True)
    v = x.var(dim=axis, keepdim=True, unbiased=False)
    return (x - m) / torch.sqrt(v + eps)
def _scatter_add(tgt, idx, src, axis=0):
    return tgt.clone().index_add_(axis, idx, src)
def _segment_sum(src, seg, n):
    out = torch.zeros((n,) + src.shape[1:], dtype=src.dtype)
    return out.index_add_(0, seg, src)
def _pad(x, pw):
    flat = tuple(v for pair in reversed(pw) for v in pair)
    return torch.nn.functional.pad(x, flat)

kernel_name = 'parallel_sum__ssm_conv_s5__mixer_token_mlp__prompt'

def setup_inputs():

    _w = (lambda a: (lambda t: t.to(torch.bfloat16) if t.is_floating_point() else t)(torch.as_tensor(a)))
    def _A_setup():
        B, S, D = 1, 2048, 576
        P = D
        Lambda_re = -0.5 * (lambda _x: torch.exp(torch.as_tensor(_x)))((lambda *s: torch.randn(*s, dtype=torch.bfloat16) * 0.2)(P))
        Lambda_im = (lambda *s: torch.randn(*s, dtype=torch.bfloat16) * 0.8)(P) * 1.0
        B_re = (lambda *s: torch.randn(*s, dtype=torch.bfloat16) * 0.2)(P, D) * (D ** -0.5)
        B_im = (lambda *s: torch.randn(*s, dtype=torch.bfloat16) * 0.2)(P, D) * (D ** -0.5)
        C_re = (lambda *s: torch.randn(*s, dtype=torch.bfloat16) * 0.2)(D, P) * (P ** -0.5)
        C_im = (lambda *s: torch.randn(*s, dtype=torch.bfloat16) * 0.2)(D, P) * (P ** -0.5)
        log_dt = (lambda *s: torch.randn(*s, dtype=torch.bfloat16) * 0.2)(P) * 0.3 - 2.0
        Dskip = (lambda *s: torch.randn(*s, dtype=torch.bfloat16) * 0.05)(D)
        return {
            "x": _w((lambda *s: torch.randn(*s, dtype=torch.bfloat16) * 0.05)(B, S, D)),
            "Lambda_re": _w(Lambda_re),
            "Lambda_im": _w(Lambda_im),
            "B_re": _w(B_re),
            "B_im": _w(B_im),
            "C_re": _w(C_re),
            "C_im": _w(C_im),
            "log_dt": _w(log_dt),
            "Dskip": _w(Dskip),
        }
    _A_in = _A_setup()

    def _B_setup():
        B, S, D, F = 1, 2048, 576, 4608
        return {
            "x": _w((lambda *s: torch.randn(*s, dtype=torch.bfloat16) * 0.05)(B, S, D)),
            "w1": _w((lambda *s: torch.randn(*s, dtype=torch.bfloat16) * 0.8)(S, F) * (S ** -0.5)),
            "b1": _w((lambda *s: torch.randn(*s, dtype=torch.bfloat16) * 3.2)(F) * 0.02),
            "w2": _w((lambda *s: torch.randn(*s, dtype=torch.bfloat16) * 0.4)(F, S) * (F ** -0.5)),
            "b2": _w((lambda *s: torch.randn(*s, dtype=torch.bfloat16) * 3.2)(S) * 0.02),
        }
    _B_in = _B_setup()

    return {
        "A_x": _A_in["x"],
        "A_Lambda_re": _A_in["Lambda_re"],
        "A_Lambda_im": _A_in["Lambda_im"],
        "A_B_re": _A_in["B_re"],
        "A_B_im": _A_in["B_im"],
        "A_C_re": _A_in["C_re"],
        "A_C_im": _A_in["C_im"],
        "A_log_dt": _A_in["log_dt"],
        "A_Dskip": _A_in["Dskip"],
        "B_x": _B_in["x"],
        "B_w1": _B_in["w1"],
        "B_b1": _B_in["b1"],
        "B_w2": _B_in["w2"],
        "B_b2": _B_in["b2"],
    }

def reference(A_x, A_Lambda_re, A_Lambda_im, A_B_re, A_B_im, A_C_re, A_C_im, A_log_dt, A_Dskip, B_x, B_w1, B_b1, B_w2, B_b2):
    def _A_ref(x, Lambda_re, Lambda_im, B_re, B_im, C_re, C_im, log_dt, Dskip):
        Bsz, L, H = x.shape
        Lam = Lambda_re + 1j * Lambda_im
        Bmat = B_re + 1j * B_im
        Cmat = C_re + 1j * C_im
        dt = torch.exp(log_dt)
        Lam_bar = torch.exp(dt * Lam)
        B_bar = ((Lam_bar - 1.0) / Lam)[:, None] * Bmat
        Bu = torch.einsum("bld,pd->blp", x + 0j, B_bar)
        h = torch.zeros_like(Bu[:, 0, :])
        hs = []
        for t in range(L):
            h = Lam_bar[None, :] * h + Bu[:, t, :]
            hs.append(h)
        hseq = torch.stack(hs, 1)
        y = 2.0 * torch.einsum("blp,dp->bld", hseq, Cmat).real + x * Dskip[None, None, :]
        return y

    def _B_ref(x, w1, b1, w2, b2):
        B, S, D = x.shape
        h = _layernorm(x, axis=-1)
        u = torch.einsum("bsd,sf->bdf", h, w1) + b1
        u = _gelu(u)
        y = torch.einsum("bdf,fs->bds", u, w2) + b2
        return (lambda x, *d: x.permute(*d))(y, 0, 2, 1)

    x = A_x
    xn = _layernorm(x, axis=-1)
    return x + _A_ref(xn, A_Lambda_re, A_Lambda_im, A_B_re, A_B_im, A_C_re, A_C_im, A_log_dt, A_Dskip) + _B_ref(xn, B_w1, B_b1, B_w2, B_b2)
```

```cpp
#define STAMPS 1
#define SMASK 1
#include <hip/hip_runtime.h>
#include <stdint.h>

typedef unsigned short u16;
typedef short s4 __attribute__((ext_vector_type(4)));
typedef __bf16 b2 __attribute__((ext_vector_type(2)));
typedef __bf16 b8 __attribute__((ext_vector_type(8)));
typedef float f2 __attribute__((ext_vector_type(2)));
typedef float f4 __attribute__((ext_vector_type(4)));
typedef unsigned u2 __attribute__((ext_vector_type(2)));
typedef unsigned u4 __attribute__((ext_vector_type(4)));
#define GAS __attribute__((address_space(1)))
#define LAS __attribute__((address_space(3)))
typedef GAS unsigned gu32;
typedef __amdgpu_buffer_rsrc_t rsrc_t;
#define RLX_AGENT __ATOMIC_RELAXED, __HIP_MEMORY_SCOPE_AGENT

constexpr int D_ = 576, F_ = 4608;

constexpr int L_HB = 0, L_DN = 32, L_FL = 64, L_HF = 320, L_END = 384;
constexpr int F_S = 1, F_U = 2, F_X = 3;
constexpr unsigned OFF_E    = 65536;
constexpr unsigned OFF_STAT = OFF_E + 16 * 576 * 16;
constexpr unsigned OFF_HF   = 1u << 20;
constexpr unsigned OFF_PF   = OFF_HF + 64 * 36 * 1024;
constexpr unsigned OFF_HS   = OFF_PF + 180 * 36 * 1024;
constexpr unsigned OFF_SLAB = OFF_HS + 1152 * 2048 * 2;
constexpr unsigned WS_END   = OFF_SLAB + 64 * 16 * 9216;

constexpr int SLOT = 34816, WOFS = 18432;
constexpr int LD_HT = 4 * SLOT;
constexpr int LD_ST = LD_HT + 9216;
constexpr int LD_LAM = LD_ST + 1024;
constexpr int LD_BS = LD_LAM + 512;
constexpr int LD_SCR = LD_BS + 512;
constexpr int LD_SY = LD_SCR + 256;
constexpr int LDS_BYTES = LD_SY + 64;
enum { Y_LN = 0, Y_GO, Y_SD, Y_HOK, Y_PAR, Y_STOP };

__device__ __forceinline__ float bflo(unsigned v) { return __uint_as_float(v << 16); }
__device__ __forceinline__ float bfhi(unsigned v) { return __uint_as_float(v & 0xffff0000u); }
__device__ __forceinline__ unsigned pk2(float a, float b) {
  f2 z = {a, b}; b2 r = __builtin_convertvector(z, b2); return __builtin_bit_cast(unsigned, r);
}
__device__ __forceinline__ float rb(float a) { return bflo(pk2(a, 0.f)); }
template <int CTRL> __device__ __forceinline__ float dpp(float e) {
  return __builtin_bit_cast(float, __builtin_amdgcn_update_dpp(0, __builtin_bit_cast(int, e), CTRL, 0xF, 0xF, true));
}
__device__ __forceinline__ float swp(float e) { return dpp<0xB1>(e); }
__device__ __forceinline__ float rdl(float v, int l) {
  return __builtin_bit_cast(float, __builtin_amdgcn_readlane(__builtin_bit_cast(int, v), l));
}
__device__ __forceinline__ float wave_sum(float v) {
  v += dpp<0xB1>(v); v += dpp<0x4E>(v); v += dpp<0x141>(v); v += dpp<0x140>(v);
  return (rdl(v, 0) + rdl(v, 16)) + (rdl(v, 32) + rdl(v, 48));
}
__device__ __forceinline__ void glds16(const char* g, char* l) {
  __builtin_amdgcn_global_load_lds((const GAS void*)g, (LAS void*)l, 16, 0, 0);
}
__device__ __forceinline__ void glds16c(const char* g, char* l) {
  __builtin_amdgcn_global_load_lds((const GAS void*)g, (LAS void*)l, 16, 0, 16);
}
template <int IMM, int AUX>
__device__ __forceinline__ void blds(rsrc_t rs, char* l, unsigned voff, unsigned soff) {
  __builtin_amdgcn_raw_ptr_buffer_load_lds(rs, (LAS void*)l, 16, voff, soff, IMM, AUX);
}
template <int AUX>
__device__ __forceinline__ void blds4(int q, rsrc_t rs, char* l, unsigned voff, unsigned soff) {
  if (q == 0) blds<0, AUX>(rs, l, voff, soff);
  else if (q == 1) blds<1024, AUX>(rs, l, voff, soff);
  else if (q == 2) blds<2048, AUX>(rs, l, voff, soff);
  else blds<3072, AUX>(rs, l, voff, soff);
}
__device__ __forceinline__ void touch(const char* g, char* l) {
  __builtin_amdgcn_global_load_lds((const GAS void*)g, (LAS void*)l, 4, 0, 0);
}
__device__ __forceinline__ b8 ld_frag(const char* p) { return *(const LAS b8*)(LAS const char*)p; }
__device__ __forceinline__ b8 ld_tr(const char* p0, const char* p1) {
  s4 a = __builtin_amdgcn_ds_read_tr16_b64_v4i16((LAS s4*)(LAS char*)p0);
  s4 b = __builtin_amdgcn_ds_read_tr16_b64_v4i16((LAS s4*)(LAS char*)p1);
  return __builtin_bit_cast(b8, __builtin_shufflevector(a, b, 0, 1, 2, 3, 4, 5, 6, 7));
}
#define ST16(v, rs, off) __builtin_amdgcn_raw_buffer_store_b128(v, rs, off, 0, 16)
#define ST8(v, rs, off) __builtin_amdgcn_raw_buffer_store_b64(v, rs, off, 0, 16)
#define MFMA(a, b, c) __builtin_amdgcn_mfma_f32_16x16x32_bf16(a, b, c, 0, 0, 0)
#define BAR() asm volatile("s_barrier" ::: "memory")
#define WAITV(n) asm volatile("s_waitcnt vmcnt(%0)" ::"n"(n) : "memory")
#define WAITL0() asm volatile("s_waitcnt lgkmcnt(0)" ::: "memory")
#define SCHED() __builtin_amdgcn_sched_barrier(0)
#define SPIN_CAP (1u << 20)
constexpr size_t OFF_DBG = 200u << 20;
#define STAMP(i) do { if (((SMASK >> (i)) & 1) && (threadIdx.x & 63) == 0) { ((unsigned long long*)((char*)ws + OFF_DBG))[(blockIdx.x * 8 + (threadIdx.x >> 6)) * 16 + (i)] = __builtin_amdgcn_s_memrealtime(); \
  ((unsigned long long*)((char*)ws + OFF_DBG + (4u << 20)))[(blockIdx.x * 8 + (threadIdx.x >> 6)) * 16 + (i)] = __builtin_amdgcn_s_memtime(); } } while (0)
#define STAMPR(i)

__device__ __forceinline__ unsigned lds_addr(const char* p) { return (unsigned)(size_t)(LAS const char*)p; }
__device__ __forceinline__ unsigned lds_get(unsigned a) {
  unsigned v;
  asm volatile("ds_read_b32 %0, %1\n\ts_waitcnt lgkmcnt(0)" : "=v"(v) : "v"(a) : "memory");
  return v;
}
__device__ __forceinline__ void lds_put(unsigned a, unsigned v) { asm volatile("ds_write_b32 %0, %1" :: "v"(a), "v"(v) : "memory"); }
__device__ __forceinline__ void lds_wait(unsigned a, unsigned need) {
  for (unsigned spins = 0; spins < (SPIN_CAP << 4); spins++)
    if (__all(lds_get(a) >= need)) break;
}
__device__ __forceinline__ void lds_idle(unsigned a, unsigned need) {
  for (unsigned spins = 0; spins < (SPIN_CAP << 4); spins++) {
    if (__all(lds_get(a) >= need)) break;
    __builtin_amdgcn_s_sleep(2);
  }
}
__device__ __forceinline__ unsigned sy(char* smem, int y) { return lds_addr(smem + LD_SY + y * 4); }
__device__ __forceinline__ void bump(char* smem, int y, int lane) {
  if (lane == 0) asm volatile("s_waitcnt lgkmcnt(0)\n\tds_add_u32 %0, %1" :: "v"(sy(smem, y)), "v"(1u) : "memory");
}

__device__ __forceinline__ gu32* ctl(char* ws, int line) { return (gu32*)ws + line * 32; }
__device__ __forceinline__ gu32* flag(char* ws, int blk, int f) { return ctl(ws, L_FL + blk) + f; }
__device__ __forceinline__ void raise(char* ws, int f, int w, int lane) {
  if (w == 0 && lane == 0) __hip_atomic_store(flag(ws, blockIdx.x, f), 1u, RLX_AGENT);
}
__device__ __forceinline__ int chan_of(int L) { return (L & ~15) + 4 * ((L & 7) >> 1) + 2 * ((L >> 3) & 1) + (L & 1); }
__device__ __forceinline__ void cstep(float& xr, float& xi, float lr, float li, float er, float ei) {
  float tr, ti;
  asm("v_fma_f32 %0, %4, %2, %6\n\tv_fma_f32 %1, %4, %3, %7\n\tv_fma_f32 %0, -%5, %3, %0\n\tv_fma_f32 %1, %5, %2, %1"
      : "=&v"(tr), "=&v"(ti) : "v"(xr), "v"(xi), "v"(lr), "v"(li), "v"(er), "v"(ei));
  xr = tr; xi = ti;
}
__device__ __forceinline__ int blk_g1(int t) { return (t % 27) * 8 + t / 27; }
__device__ __forceinline__ int blk_scan(int rc, int cg) { return ((cg % 6) * 4 + (rc & 3)) * 8 + (rc >> 2) * 2 + cg / 6; }
__device__ __forceinline__ const gu32* state_flag(char* ws, int st, int dq, int lane) {
  const int k = lane < 21 ? lane : 0;
  return flag(ws, k < 12 ? blk_scan(st, k) : 216 + dq * 9 + (k - 12), F_S);
}
template <int NAP>
__device__ __forceinline__ void wait_ge(const gu32* p, unsigned need) {
  for (unsigned spins = 0; spins < SPIN_CAP; spins++) {
    const unsigned v = __hip_atomic_load(p, RLX_AGENT);
    if (__all(v >= need)) break;
    __builtin_amdgcn_s_sleep(NAP);
  }
  __builtin_amdgcn_fence(__ATOMIC_ACQUIRE, "wavefront");
}

__device__ __forceinline__ unsigned sld(const char* p) {
  unsigned v;
  asm volatile("s_load_dword %0, %1, 0x0 glc\n\ts_waitcnt lgkmcnt(0)" : "=s"(v) : "s"(p) : "memory");
  return v;
}

__device__ __forceinline__ void spf(const char* p) {
  asm volatile("s_load_dword s90, %0, 0x0\n\ts_load_dword s90, %0, 0x40\n\ts_load_dword s90, %0, 0x80\n\ts_load_dword s90, %0, 0xc0\n\t"
               "s_load_dword s90, %0, 0x100\n\ts_load_dword s90, %0, 0x140\n\ts_load_dword s90, %0, 0x180\n\ts_load_dword s90, %0, 0x1c0"
               :: "s"(p) : "s90", "memory");
}
#define TAILT 2
#define TAILA 1
#define UNAP 3
#define SP_X 44
constexpr int TL_ROWS = 27 * 2 * TAILT * 4, SP0 = 1152 - 6 * SP_X, PF_W2 = (SP0 - TL_ROWS) / 4;
__device__ __forceinline__ const char* spf_base(const u16* w2, int row0) {
  const int b = blockIdx.x, l = b >> 3;
  return (const char*)w2 + (size_t)(((b & 7) >> 1) * 1152 + row0 + (l >> 2)) * 4096 + (b & 1) * 2048 + (l & 3) * 512;
}

__device__ __forceinline__ void doze(const gu32* f) {
  for (unsigned spins = 0; spins < SPIN_CAP && sld((const char*)f) == 0u; spins++) __builtin_amdgcn_s_sleep(8);
}

__device__ __forceinline__ float gelu_f(float u) {
  const float c0 = -2.f * 0.7978845608028654f * 1.4426950408889634f;
  const float t = __builtin_fmaf(u * u, c0 * 0.044715f, c0);
  const float e = __builtin_amdgcn_exp2f(u * t);
  return u * __builtin_amdgcn_rcpf(1.f + e);
}

struct Args {
  const u16 *x, *lre, *lim, *bre, *bim, *cre, *cim, *logdt, *dskip, *w1, *b1, *w2, *b2;
  char* ws; float* out;
};

#define BNAP 40
#define HARR 1
template <int NP, bool WFIRST>
struct Tile {
  f4 acc[2][NP];
  b8 P0[NP], W0[2], P1[NP], W1[2];
  int wofs[2], pofs;
  __device__ __forceinline__ void rd(const char* sl, int ks, b8* P, b8* W) {
#pragma unroll
    for (int a = 0; a < 2; a++) W[a] = ld_tr(sl + wofs[a] + ks * 8192, sl + wofs[a] + ks * 8192 + 4096);
#pragma unroll
    for (int i = 0; i < NP; i++) P[i] = ld_frag(sl + pofs + ks * 9216 + i * 1024);
  }
  __device__ __forceinline__ void mm(const b8* P, const b8* W) {
#pragma unroll
    for (int i = 0; i < NP; i++)
#pragma unroll
      for (int a = 0; a < 2; a++) acc[a][i] = WFIRST ? MFMA(W[a], P[i], acc[a][i]) : MFMA(P[i], W[a], acc[a][i]);
#pragma unroll
    for (int r = 0; r < 4 + NP; r++) {
      __builtin_amdgcn_sched_group_barrier(0x8, 1, 0);
      __builtin_amdgcn_sched_group_barrier(0x100, 1, 0);
    }
    __builtin_amdgcn_sched_group_barrier(0x8, 2 * NP, 0);
  }
  template <class G>
  __device__ __forceinline__ void half(const char* sl, int ks, const b8* P, const b8* W, b8* Pn, b8* Wn, int t, G side) {
#pragma unroll
    for (int i = 0; i < NP; i++) {
#pragma unroll
      for (int a = 0; a < 2; a++) acc[a][i] = WFIRST ? MFMA(W[a], P[i], acc[a][i]) : MFMA(P[i], W[a], acc[a][i]);
      if (i < 2) Wn[i] = ld_tr(sl + wofs[i] + ks * 8192, sl + wofs[i] + ks * 8192 + 4096);
      else if (2 * i - 4 < NP) {
        Pn[2 * i - 4] = ld_frag(sl + pofs + ks * 9216 + (2 * i - 4) * 1024);
        Pn[2 * i - 3] = ld_frag(sl + pofs + ks * 9216 + (2 * i - 3) * 1024);
      }
      __builtin_amdgcn_sched_group_barrier(0x8, 1, 0); __builtin_amdgcn_sched_group_barrier(0x100, 1, 0);
      __builtin_amdgcn_sched_group_barrier(0x8, 1, 0); __builtin_amdgcn_sched_group_barrier(0x100, 1, 0);
      SCHED();
      side(t, i);
      SCHED();
    }
  }
  __device__ __forceinline__ void first(const char* smem) { rd(smem, 0, P0, W0); }
  template <bool DRAIN = false, class F>
  __device__ __forceinline__ void step(const char* smem, int u, F mid) {
    SCHED();
    rd(smem + u * SLOT, 1, P1, W1);
    mm(P0, W0);
    SCHED();
    if (DRAIN) WAITV(0);
    WAITL0();
    BAR();
    mid();
    SCHED();
    rd(smem + ((u + 1) & 3) * SLOT, 0, P0, W0);
    mm(P1, W1);
    SCHED();
  }
  template <class G>
  __device__ __forceinline__ void step_side(const char* smem, int u, G side) {
    SCHED();
    half(smem + u * SLOT, 1, P0, W0, P1, W1, 2 * u, side);
    WAITL0();
    BAR();
    SCHED();
    half(smem + ((u + 1) & 3) * SLOT, 0, P1, W1, P0, W0, 2 * u + 1, side);
  }
};
#define DPP_SWAP " quad_perm:[1,0,3,2] row_mask:0xf bank_mask:0xf"

__device__ __forceinline__ void consumer(const Args& A, char* smem, int lane, int w) {
  char* ws = A.ws;
  const int b = blockIdx.x, xcd = b & 7, l = b >> 3;
  const int g = lane >> 4, c = lane & 15, q = c >> 2, p = lane & 3;
  const rsrc_t rs = __builtin_amdgcn_make_buffer_rsrc(ws, 0, WS_END, 0x00020000);
  const bool tA = l < 24, tG1 = l < 27;
  STAMP(0);
  const int t1 = xcd * 27 + (tG1 ? l : 0), ft = t1 / 6, mt = t1 % 6;
  u2 bias1[2];
  {
    const int s0 = (b >> 2) * 32 + (b & 3) * 4 + (w & 1) * 2 + (w >> 1) * 16;
    unsigned xr[2][9];
#pragma unroll
    for (int r = 0; r < 2; r++)
#pragma unroll
      for (int i = 0; i < 9; i++) xr[r][i] = A.x[(s0 + r) * D_ + i * 64 + lane];
#pragma unroll
    for (int a = 0; a < 2; a++) bias1[a] = *(const u2*)(A.b1 + ft * 128 + (2 * w + a) * 16 + 4 * g);
    unsigned sh16;
    asm volatile("s_waitcnt lgkmcnt(0)\n\ts_barrier\n\tv_mov_b32 %0, 16" : "=v"(sh16) :: "memory");
    f2 v[9], a = {0.f, 0.f};
#pragma unroll
    for (int i = 0; i < 9; i++) { v[i] = (f2){__uint_as_float(xr[0][i] << sh16), __uint_as_float(xr[1][i] << sh16)}; a += v[i]; }
    const f2 m = (f2){wave_sum(a[0]), wave_sum(a[1])} * (1.f / D_);
    f2 qq = {0.f, 0.f};
#pragma unroll
    for (int i = 0; i < 9; i++) { const f2 d = v[i] - m; qq += d * d; }
    const f2 var = (f2){wave_sum(qq[0]), wave_sum(qq[1])} * (1.f / D_);
    f2 mb, r1, sc, sh;
#pragma unroll
    for (int r = 0; r < 2; r++) {
      mb[r] = rb(m[r]);
      r1[r] = __builtin_amdgcn_rcpf(rb(__builtin_amdgcn_sqrtf(rb(rb(var[r]) + 1e-5f))));
      const float mb2 = rb((m[r] - mb[r]) * r1[r]);
      const float r2 = __builtin_amdgcn_rcpf(rb(__builtin_amdgcn_sqrtf(rb(rb(var[r] * r1[r] * r1[r]) + 1e-5f))));
      sc[r] = r1[r] * r2; sh[r] = -mb2 * r2;
    }
#pragma unroll
    for (int i = 0; i < 9; i++) {
      const f2 h = (v[i] - mb) * sc + sh;
      *(unsigned*)(smem + LD_HT + (i * 64 + lane) * 16 + w * 4) = pk2(h[0], h[1]);
    }
    if (lane == 0)
      ST16(((u4){__float_as_uint(mb[0]), __float_as_uint(r1[0]), __float_as_uint(mb[1]), __float_as_uint(r1[1])}), rs,
           OFF_STAT + s0 * 8);
  }
  STAMP(1);
  if (tA) { WAITL0(); BAR(); }
  else { bump(smem, Y_LN, lane); lds_wait(sy(smem, Y_LN), 4u); }
#pragma unroll
  for (int r = 0; r < 3; r++) {
    const int d = r * 256 + w * 64 + lane;
    if (r < 2 || w == 0)
      ST16(*(const u4*)(smem + LD_HT + d * 16), rs, OFF_HF + ((b >> 2) * 36 + (d >> 4)) * 1024 + ((b & 3) * 16 + (d & 15)) * 16);
  }
  STAMP(2);
  f4 bu[2][6];
  const int rc = (xcd >> 1) * 4 + (l & 3), cg = (xcd & 1) * 6 + (l >> 2);
  if (tA) {
    f4 bs[2] = {{0.f, 0.f, 0.f, 0.f}, {0.f, 0.f, 0.f, 0.f}};
#pragma unroll
    for (int i = 0; i < 2; i++)
#pragma unroll
      for (int n = 0; n < 6; n++) bu[i][n] = (f4){0.f, 0.f, 0.f, 0.f};
    const u4 one4 = {0x3f803f80u, 0x3f803f80u, 0x3f803f80u, 0x3f803f80u};
    const b8 ones = __builtin_bit_cast(b8, one4);
    int fo[2];
#pragma unroll
    for (int ks = 0; ks < 2; ks++) fo[ks] = c * 128 + (((ks * 4 + g) ^ (c >> 1)) << 4);
    b8 X0[2], B0[8], X1[2], B1[8];
    auto rd = [&](const char* sl, int ks, b8* X, b8* B) {
#pragma unroll
      for (int i = 0; i < 2; i++) X[i] = ld_frag(sl + fo[ks] + w * 4096 + i * 2048);
#pragma unroll
      for (int n = 0; n < 6; n++) B[n] = ld_frag(sl + fo[ks] + 16384 + n * 2048);
      B[6] = ld_frag(sl + fo[ks] + 16384 + w * 2048);
      B[7] = ld_frag(sl + fo[ks] + 16384 + (4 + (w & 1)) * 2048);
    };
    auto mm = [&](const b8* X, const b8* B) {
#pragma unroll
      for (int n = 0; n < 6; n++)
#pragma unroll
        for (int i = 0; i < 2; i++) bu[i][n] = MFMA(X[i], B[n], bu[i][n]);
      bs[0] = MFMA(ones, B[6], bs[0]);
      bs[1] = MFMA(ones, B[7], bs[1]);
#pragma unroll
      for (int r = 0; r < 11; r++) {
        __builtin_amdgcn_sched_group_barrier(0x8, 1, 0);
        __builtin_amdgcn_sched_group_barrier(0x100, 1, 0);
      }
      __builtin_amdgcn_sched_group_barrier(0x8, 14, 0);
    };
    rd(smem + 3 * SLOT, 0, X0, B0);
#pragma unroll
    for (int s = 0; s < 9; s++) {
      SCHED();
      rd(smem + ((s + 3) & 3) * SLOT, 1, X1, B1);
      mm(X0, B0);
      SCHED();
      if (s < 8) {
        if (s == HARR) WAITV(0);
        WAITL0();
        BAR();
        SCHED();
        rd(smem + (s & 3) * SLOT, 0, X0, B0);
      }
      mm(X1, B1);
      SCHED();
    }
    if (lane < 16) {
      *(float*)(smem + LD_BS + (w * 16 + c) * 4) = bs[0][0];
      *(float*)(smem + LD_BS + ((4 + (w & 1)) * 16 + c) * 4) = bs[1][0];
    }
  } else {
    WAITV(0);
    bump(smem, Y_LN, lane);
    const int fb = (l - 27) * 8 + xcd, kg = fb % 9, du = fb / 9;
    if (!tG1 && fb < 36) {
      lds_idle(sy(smem, (lane & 1) ? Y_GO : Y_PAR), 1u);
      u4 cr[3][2], ci[3][2];
#pragma unroll
      for (int r = 0; r < 3; r++) {
        const int o = ((du * 9 + (w + 4 * r < 9 ? w + 4 * r : w)) * 16 + c) * D_ + kg * 64 + g * 8;
#pragma unroll
        for (int h = 0; h < 2; h++) { cr[r][h] = *(const u4*)(A.cre + o + h * 32); ci[r][h] = *(const u4*)(A.cim + o + h * 32); }
      }
#pragma unroll
      for (int h = 0; h < 2; h++) {
        f4 ff[4];
#pragma unroll
        for (int k = 0; k < 4; k++) ff[k] = *(const f4*)(smem + LD_LAM + (h * 32 + g * 8 + 2 * k) * 8);
#pragma unroll
        for (int r = 0; r < 3; r++) {
          if (w + 4 * r < 9) {
#pragma unroll
            for (int e = 0; e < 2; e++) {
              u4 o;
#pragma unroll
              for (int t = 0; t < 2; t++) {
                const unsigned a = cr[r][h][2 * e + t], bq = ci[r][h][2 * e + t];
                const f4 f = ff[2 * e + t];
                o[t * 2] = pk2(bflo(a) * f[0] - bflo(bq) * f[1], -(bflo(a) * f[1] + bflo(bq) * f[0]));
                o[t * 2 + 1] = pk2(bfhi(a) * f[2] - bfhi(bq) * f[3], -(bfhi(a) * f[3] + bfhi(bq) * f[2]));
              }
              ST16(o, rs, OFF_PF + ((144 + kg * 4 + h * 2 + (g >> 1)) * 36 + du * 9 + w + 4 * r) * 1024 + (((g & 1) * 2 + e) * 16 + c) * 16);
            }
          }
        }
      }
      WAITV(0);
      bump(smem, Y_SD, lane);
    }
  }
  STAMP(3);
  const int wW[2] = {WOFS + (4 * g + q) * 256 + (((2 * w) ^ (4 * (g & 1) + q)) * 32) + p * 8,
                     WOFS + (4 * g + q) * 256 + (((2 * w + 1) ^ (4 * (g & 1) + q)) * 32) + p * 8};
  if (tG1) {
    Tile<6, true> T;
    T.wofs[0] = wW[0]; T.wofs[1] = wW[1]; T.pofs = lane * 16;
#pragma unroll
    for (int a = 0; a < 2; a++) {
      const u2 bv = bias1[a];
#pragma unroll
      for (int i = 0; i < 6; i++) T.acc[a][i] = (f4){bflo(bv[0]), bfhi(bv[0]), bflo(bv[1]), bfhi(bv[1])};
    }
    WAITL0();
    BAR();
    STAMP(4);
    T.first(smem);
    float* agg = (float*)(smem + LD_HT) + (w * 4 + g) * 96 + c;
    auto nop = []() {};
    float ar[6], ai[6], hv[8][6];
    auto lam = [&]() {
      const float sg = (c & 1) ? 1.f : -1.f;
#pragma unroll
      for (int n = 0; n < 6; n++) {
        const f2 lm = *(const f2*)(smem + LD_LAM + (n * 8 + (c >> 1)) * 8);
        ar[n] = lm[0]; ai[n] = lm[1] * sg;
      }
    };
    auto plain = [&](int rounds) {
#pragma nounroll
      for (int r = 0; r < rounds; r++) {
        T.step(smem, 0, nop); T.step(smem, 1, nop);
        T.template step<true>(smem, 2, nop);
        T.step(smem, 3, nop);
      }
    };
    if (tA) {
      float bsum[6], e[6];
      f4 stt[4];
#pragma unroll
      for (int t = 0; t < 4; t++) stt[t] = *(const f4*)(smem + LD_ST + (w * 32 + g * 8 + 2 * t) * 8);
#pragma unroll
      for (int n = 0; n < 6; n++) { bsum[n] = *(const float*)(smem + LD_BS + (n * 16 + c) * 4); e[n] = 0.f; }
      lam();
      auto p1 = [&](int t, int n) {
        float en;
        hv[t][n] = bu[t >> 2][n][t & 3];
        asm volatile("v_fma_f32 %0, -%2, %4, %0\n\tv_mul_f32 %0, %0, %3\n\tv_fma_f32 %1, %5, %7, %0\n\tv_fmac_f32_dpp %1, %7, %6" DPP_SWAP
                     : "+v"(hv[t][n]), "=&v"(en)
                     : "v"(stt[t >> 1][(t & 1) * 2]), "v"(stt[t >> 1][(t & 1) * 2 + 1]), "v"(bsum[n]), "v"(ar[n]), "v"(ai[n]), "v"(e[n]));
        e[n] = en;
      };
      T.step_side(smem, 0, p1); T.step_side(smem, 1, p1); T.step_side(smem, 2, p1); T.step_side(smem, 3, p1);
#pragma unroll
      for (int n = 0; n < 6; n++) agg[n * 16] = e[n];
    }
    STAMP(5);
    plain(tA ? 3 : 4);
    STAMPR(3);
    if (tA) {
      float e[6];
      lam();
#pragma unroll
      for (int n = 0; n < 6; n++) e[n] = agg[n * 16];
      auto p2 = [&](int t, int n) {
        asm volatile("v_fma_f32 %0, %1, %3, %0\n\tv_fmac_f32_dpp %0, %3, %2" DPP_SWAP
                     : "+v"(hv[t][n]) : "v"(ar[n]), "v"(ai[n]), "v"(t ? hv[t ? t - 1 : 0][n] : e[n]));
      };
      T.step_side(smem, 0, p2); T.step_side(smem, 1, p2); T.step_side(smem, 2, p2); T.step_side(smem, 3, p2);
      const unsigned hb = OFF_HS + rc * (36 * 8192) + cg * 6 * 4096 + (2 * w + (g >> 1)) * 512 + (g & 1) * 256 +
                          ((q ^ (2 * (g & 1))) * 64) + (c & 3) * 16;
#pragma unroll
      for (int n = 0; n < 6; n++)
        ST16(((u4){pk2(hv[0][n], hv[1][n]), pk2(hv[2][n], hv[3][n]), pk2(hv[4][n], hv[5][n]), pk2(hv[6][n], hv[7][n])}),
             rs, hb + n * 4096);
    }
    STAMPR(4);
    plain(tA ? 3 : 4);
    STAMP(6);
    const unsigned ub = OFF_PF + ((ft * 4 + w) * 36 + mt * 6) * 1024 + lane * 16;
#pragma unroll
    for (int i = 0; i < 6; i++)
      ST16(((u4){pk2(gelu_f(T.acc[0][i][0]), gelu_f(T.acc[0][i][1])), pk2(gelu_f(T.acc[0][i][2]), gelu_f(T.acc[0][i][3])),
                 pk2(gelu_f(T.acc[1][i][0]), gelu_f(T.acc[1][i][1])), pk2(gelu_f(T.acc[1][i][2]), gelu_f(T.acc[1][i][3]))}),
           rs, ub + i * 1024);
  }
  STAMP(7);
  const int j = xcd >> 1, st = (xcd & 1) * 8 + (l >> 2), dq = l & 3, tile = st * 4 + dq;
  Tile<9, false> T;
  T.pofs = lane * 16;
#pragma unroll
  for (int a = 0; a < 2; a++) {
    T.wofs[a] = WOFS + (2 * w + a) * 512 + (p >> 1) * 256 + ((g ^ (p & 2)) * 64) + q * 16 + (p & 1) * 8;
#pragma unroll
    for (int i = 0; i < 9; i++) T.acc[a][i] = (f4){0.f, 0.f, 0.f, 0.f};
  }
  BAR();
  STAMP(8);
  T.first(smem);
  {
    auto nop = []() {};
    int wn[2];
    auto sw = [&]() { asm volatile("v_mov_b32 %0, %2\n\tv_mov_b32 %1, %3" : "=&v"(T.wofs[0]), "=&v"(T.wofs[1]) : "v"(wn[0]), "v"(wn[1])); };
#pragma nounroll
    for (int r = 0; r < 5; r++) {
      const bool wt0 = r > 0, wt3 = r > 0 || j >= 2;
      wn[0] = wt0 ? wW[0] : T.wofs[0]; wn[1] = wt0 ? wW[1] : T.wofs[1];
      T.template step<true>(smem, 0, [&]() { sw(); if (r == 0 && tG1) raise(ws, F_U, w, lane); });
      T.step(smem, 1, nop); T.step(smem, 2, nop);
      wn[0] = wt3 ? wW[0] : T.wofs[0]; wn[1] = wt3 ? wW[1] : T.wofs[1];
      T.step(smem, 3, sw);
      STAMPR(8 + r);
    }
    T.step(smem, 0, nop); T.step(smem, 1, nop);
    if (j < 2) T.step(smem, 2, nop);
  }
  STAMP(9);
  char* own = smem;
  if (w != j) {
    const unsigned o = OFF_SLAB + ((tile * 4 + w) * 4 + j) * 9216 + lane * 16;
#pragma unroll
    for (int i = 0; i < 9; i++)
      ST16(((u4){pk2(T.acc[0][i][0], T.acc[0][i][1]), pk2(T.acc[0][i][2], T.acc[0][i][3]),
                 pk2(T.acc[1][i][0], T.acc[1][i][1]), pk2(T.acc[1][i][2], T.acc[1][i][3])}), rs, o + i * 1024);
  } else {
#pragma unroll
    for (int a = 0; a < 2; a++)
#pragma unroll
      for (int i = 0; i < 9; i++) *(f4*)(own + (a * 9 + i) * 1024 + lane * 16) = T.acc[a][i];
  }
  f4 res[3][2];
#pragma unroll
  for (int r = 0; r < 3; r++) {
    const int i = (w + 4 * r < 9) ? w + 4 * r : w;
    const u2 dv = *(const u2*)(smem + LD_ST + 512 + i * 32 + g * 8);
#pragma unroll
    for (int a = 0; a < 2; a++) {
      const u2 xv = *(const u2*)(smem + LD_HT + (a * 16 + c) * 288 + i * 32 + g * 8);
      const f2 sv = *(const f2*)(smem + LD_ST + (a * 16 + c) * 8);
      const float bb = bflo(*(const u16*)(smem + LD_ST + 256 + (a * 16 + c) * 2));
#pragma unroll
      for (int e = 0; e < 4; e++) {
        const float xx = (e & 1) ? bfhi(xv[e >> 1]) : bflo(xv[e >> 1]);
        const float dd = (e & 1) ? bfhi(dv[e >> 1]) : bflo(dv[e >> 1]);
        res[r][a][e] = __builtin_fmaf((xx - sv[0]) * sv[1], dd, xx + bb);
      }
    }
  }
  asm volatile("s_waitcnt vmcnt(0) lgkmcnt(0)" ::: "memory");
  BAR();
  STAMP(10);
  if (w == 0) {
    raise(ws, F_X, w, lane);
    wait_ge<1>(flag(ws, (b & ~6) | ((lane & 3) << 1), F_X), 1u);
  }
  BAR();
  STAMP(11);
  u4 pv[3][3];
#pragma unroll
  for (int r = 0; r < 3; r++) {
    const int i = (w + 4 * r < 9) ? w + 4 * r : w;
#pragma unroll
    for (int k = 0; k < 3; k++) {
      const int src = (j + 1 + k) & 3;
      pv[r][k] = __builtin_amdgcn_raw_buffer_load_b128(rs, OFF_SLAB + ((tile * 4 + j) * 4 + src) * 9216 + i * 1024 + lane * 16, 0, 16);
    }
  }
#pragma unroll
  for (int r = 0; r < 3; r++) {
    if (w + 4 * r < 9) {
      const int i = w + 4 * r;
#pragma unroll
      for (int a = 0; a < 2; a++) {
        const int s = st * 128 + j * 32 + a * 16 + c, d = dq * 144 + i * 16 + 4 * g;
        f4 o = *(const f4*)(own + (a * 9 + i) * 1024 + lane * 16) + res[r][a];
#pragma unroll
        for (int k = 0; k < 3; k++)
          o += (f4){bflo(pv[r][k][2 * a]), bfhi(pv[r][k][2 * a]), bflo(pv[r][k][2 * a + 1]), bfhi(pv[r][k][2 * a + 1])};
        *(f4*)(A.out + s * D_ + d) = o;
      }
    }
  }
  STAMP(12);
  WAITV(0);
  STAMP(13);
}

#define PFMORE 4
#define PFNAP 32
#define AUXW1 0
#define AUXH 0
#define AUXW2 0
#define AUXP 0
#define PFCAP 3
#define PF_B 7
__device__ __forceinline__ void producer(const Args& A, char* smem, int lane, int pw) {
  char* ws = A.ws;
  const int b = blockIdx.x, xcd = b & 7, l = b >> 3;
  const bool tA = l < 24, tG1 = l < 27;
  const int t1 = xcd * 27 + l, ft = t1 / 6, mt = t1 % 6;
  const int r0 = lane >> 4, cc = lane & 15;
  const rsrc_t rs = __builtin_amdgcn_make_buffer_rsrc(ws, 0, WS_END, 0x00020000);
  const unsigned l16 = lane * 16;
  const rsrc_t rw = __builtin_amdgcn_make_buffer_rsrc((void*)A.w1, 0, 2048 * F_ * 2, 0x00020000);
  unsigned v1[4];
#pragma unroll
  for (int i = 0; i < 4; i++) { const int row = i * 4 + r0; v1[i] = row * (F_ * 2) + ((cc ^ ((row & 7) << 1)) << 4) - i * 1024; }
  auto is1w = [&](int k) {
    const unsigned wo = (k * 64 + pw * 32) * (F_ * 2) + ft * 256;
#pragma unroll
    for (int i = 0; i < 8; i++)
      blds4<AUXW1>(i & 3, rw, smem + (k & 3) * SLOT + WOFS + pw * 8192 + (i >> 2) * 4096, v1[i & 3], wo + (i >> 2) * 16 * (F_ * 2));
  };
  auto is1p = [&](int k) {
    const unsigned po = OFF_HF + (k * 72 + pw * 36 + mt * 6) * 1024;
#pragma unroll
    for (int i = 0; i < 6; i++) blds4<16 | AUXH>(i & 3, rs, smem + (k & 3) * SLOT + pw * 9216 + (i >> 2) * 4096, l16, po + (i >> 2) * 4096);
  };
  STAMP(0);
  if (tA) {
    const int rc = (xcd >> 1) * 4 + (l & 3), cg = (xcd & 1) * 6 + (l >> 2);
    const int rr = lane >> 3;
    const rsrc_t rx = __builtin_amdgcn_make_buffer_rsrc((void*)A.x, 0, 2048 * D_ * 2, 0x00020000);
    unsigned xv[4]; const char* ba[2];
#pragma unroll
    for (int h = 0; h < 2; h++) {
      const int c16 = h * 8 + rr, sw = ((lane & 7) ^ (c16 >> 1)) << 4;
      xv[h] = ((c16 >> 2) * 8 + (c16 & 3)) * (D_ * 2) + sw - h * 1024;
      xv[h + 2] = xv[h] + 4 * (D_ * 2) - 2048;
      ba[h] = (const char*)((rr & 1) ? A.bim : A.bre) + (size_t)(cg * 48 + chan_of(c16 >> 1)) * (D_ * 2) + sw;
    }
    int kt = (l * 3 + (l >> 2)) % 9, s = 0;
    auto issue = [&]() {
      char* sl = smem + ((s + 3) & 3) * SLOT;
#pragma unroll
      for (int i = 0; i < 8; i++)
        blds4<0>(i & 3, rx, sl + pw * 8192 + (i >> 2) * 4096, xv[i & 3], (rc * 128 + pw * 64 + (i >> 2) * 32) * (D_ * 2) + kt * 128);
#pragma unroll
      for (int i = 0; i < 6; i++)
        glds16(ba[i & 1] + chan_of((pw * 3 + (i >> 1)) * 8) * (D_ * 2) + kt * 128, sl + 16384 + pw * 6144 + i * 1024);
      kt = kt == 8 ? 0 : kt + 1; s++;
    };
    BAR();
    issue(); issue();
    WAITV(14); BAR();
    __builtin_amdgcn_s_sleep(2);
    issue();
    WAITV(14); BAR();
    issue(); issue();
#pragma nounroll
    for (int k = 2; k < 6; k++) { WAITV(28); BAR(); issue(); }
    WAITV(28); BAR();
    is1w(0);
    WAITV(22); BAR();
    is1w(1);
    WAITV(16); BAR();
  } else {
    BAR();
    const char* base = (const char*)A.w1 + xcd * 1152;
    int pc = (l - 24) * 2 + pw;
    auto tw1 = [&]() { const int li = pc * 64 + lane; touch(base + (size_t)(li / 9) * (F_ * 2) + (li % 9) * 128, smem + LD_SCR); WAITV(PFCAP); };
    if (b != 255) { tw1(); pc += 16; tw1(); pc += 16; }
    lds_idle(sy(smem, Y_GO), 1u);
#pragma nounroll
    for (; pc < 16 * PF_B; pc += 16) tw1();
    if (!tG1) {
      pc = 16 * PF_B + (l - 27) * 2 + pw;
#pragma nounroll
      for (; pc < 288; pc += 10) tw1();
    }
    if (tG1) { is1w(0); is1w(1); }
    lds_wait(sy(smem, Y_HOK), 1u);
  }
  STAMP(1);
  const int j = xcd >> 1, st = (xcd & 1) * 8 + (l >> 2), dq = l & 3;
  const int ns = j < 2 ? 5 : 4, ks0 = j < 2 ? j * 5 : 2 + j * 4;
  const rsrc_t rw2 = __builtin_amdgcn_make_buffer_rsrc((void*)A.w2, 0, F_ * 4096, 0x00020000);
  unsigned v2[4];
#pragma unroll
  for (int i = 0; i < 4; i++) { const int row = i * 4 + r0; v2[i] = row * 4096 + ((cc ^ ((row & 7) << 1)) << 4) - i * 1024; }
  auto is2p = [&](int k, int kp) {
    char* sl = smem + (k & 3) * SLOT + pw * 9216;
    const unsigned po = OFF_PF + ((kp * 2 + pw) * 36 + dq * 9) * 1024;
#pragma unroll
    for (int i = 0; i < 9; i++) blds4<16 | AUXP>(i & 3, rs, sl + (i >> 2) * 4096, l16, po + (i >> 2) * 4096);
  };
  auto is2s = [&](int k) {
    char* sl = smem + (k & 3) * SLOT + WOFS + pw * 8192;
    const unsigned ho = OFF_HS + (st * 36 + (ks0 + k) * 2 + pw) * 8192;
#pragma unroll
    for (int i = 0; i < 8; i++) blds4<16>(i & 3, rs, sl + (i >> 2) * 4096, l16, ho + (i >> 2) * 4096);
    is2p(k, 72 + ks0 + k);
  };
  auto is2w = [&](int k) {
    char* sl = smem + ((ns + k) & 3) * SLOT + WOFS + pw * 8192;
    const unsigned wo = ((j * 18 + k) * 64 + pw * 32) * 4096 + st * 256;
#pragma unroll
    for (int i = 0; i < 8; i++) blds4<AUXW2>(i & 3, rw2, sl + (i >> 2) * 4096, v2[i & 3], wo + (i >> 2) * 16 * 4096);
  };
  auto tail_touch = [&](int i0, int i1) {
#pragma unroll
    for (int i = i0; i < i1; i++)
      touch((const char*)A.w2 + (size_t)(j * 1152 + SP0 - TL_ROWS + ((i * 27 + l) * 2 + pw) * 4 + (lane >> 4)) * 4096 + (xcd & 1) * 2048 +
                (lane & 15) * 128, smem + LD_SCR);
  };
  if (tG1) {
    if (tA && pw == 0) blds<0, 16>(rs, smem + LD_ST, l16, OFF_STAT + ((xcd >> 1) * 4 + (l & 3)) * 1024);
    is1p(0); is1p(1);
    is1p(2); is1w(2);
    WAITV(20); BAR();
    is1p(3); is1w(3);
    WAITV(28); BAR();
    is1p(4); is1w(4);
#pragma nounroll
    for (int i = 5; i < 32; i++) {
      WAITV(28); BAR();
      is1p(i); is1w(i);
    }
    tail_touch(0, TAILA);
    WAITV(28 + TAILA); BAR();
    WAITV(14 + TAILA); BAR();
    WAITV(TAILA); BAR();
    BAR();
    is2s(0); is2s(1); is2s(2);
  } else {
    const char* b2 = (const char*)A.w2 + (size_t)(j * 1152 + (lane >> 4)) * 4096 + (xcd & 1) * 2048 + (lane & 15) * 128;
    int pc2 = (l - 27) * 2 + pw;
#pragma nounroll
    for (; pc2 < PF_W2 && __all(lds_get(sy(smem, Y_STOP)) == 0u); pc2 += 10) { touch(b2 + (size_t)pc2 * 4 * 4096, smem + LD_SCR); WAITV(2); }
    lds_idle(sy(smem, Y_STOP), 1u);
    STAMP(3);
    is2s(0); is2s(1); is2s(2);
  }
  WAITV(34); BAR();
  is2s(3);
  WAITV(34); BAR();
  if (j < 2) { is2s(4); if (tG1) tail_touch(TAILA, TAILT); WAITV(34); BAR(); WAITV(17); BAR(); }
  else { WAITV(17); if (tG1) tail_touch(TAILA, TAILT); BAR(); }
  is2w(0); is2w(1);
  {
    const int s0 = st * 128 + j * 32;
    const rsrc_t rx = __builtin_amdgcn_make_buffer_rsrc((void*)A.x, 0, 2048 * D_ * 2, 0x00020000);
#pragma unroll
    for (int i = 0; i < 5; i++) {
      const int pc = i * 2 + pw, ch = pc * 64 + lane;
      if (pc < 9) blds<0, 0>(rx, smem + LD_HT + pc * 1024, (ch / 18) * (D_ * 2) + (ch % 18) * 16, s0 * (D_ * 2) + dq * 288);
    }
    if (pw == 1) {
      __builtin_amdgcn_raw_ptr_buffer_load_lds(rs, (LAS void*)(smem + LD_ST), 4, lane * 4, OFF_STAT + s0 * 8, 0, 16);
      touch((const char*)(A.b2 + s0) + (lane & 15) * 4, smem + LD_ST + 256);
      touch((const char*)(A.dskip + dq * 144) + lane * 4, smem + LD_ST + 512);
      touch((const char*)(A.dskip + dq * 144) + 256 + (lane & 7) * 4, smem + LD_ST + 768);
    }
  }
  if (!tG1) { WAITV(16); BAR(); }
  if (!tA) doze(flag(ws, blk_g1(j * 54 + 12 + (dq * 3 >> 1)), F_U));
  if (pw == 0 || !tG1) {
    const int k = lane < 18 ? lane : 0;
    wait_ge<UNAP>(flag(ws, blk_g1((j * 9 + (k >> 1)) * 6 + (dq * 3 >> 1) + (k & 1)), F_U), 1u);
  }
  STAMP(4);
  if (tG1) { WAITV(16); BAR(); }
  is2p(ns, j * 18);
  is2p(ns + 1, j * 18 + 1);
  WAITV(9); BAR();
  int k2 = 2;
  auto issue2 = [&]() { is2w(k2); is2p(ns + k2, j * 18 + k2); k2++; };
  issue2();
  WAITV(17); BAR();
  issue2(); issue2();
#pragma nounroll
  for (int i = 0; i < 13; i++) { WAITV(34); BAR(); issue2(); }
  WAITV(34); BAR();
  WAITV(17); BAR();
  WAITV(0); BAR();
  BAR();
  WAITL0();
}

__device__ __forceinline__ void helper(const Args& A, char* smem, int lane) {
  char* ws = A.ws;
  const int b = blockIdx.x, xcd = b & 7, l = b >> 3;
  const bool tA = l < 24, tG1 = l < 27;
  const int rc = (xcd >> 1) * 4 + (l & 3), cg = (xcd & 1) * 6 + (l >> 2);
  const int L = lane < 48 ? lane : 47;
  const rsrc_t rs = __builtin_amdgcn_make_buffer_rsrc(ws, 0, WS_END, 0x00020000);
  const unsigned eo = OFF_E + (rc * 576 + cg * 48 + L) * 16;
  if (lane == 63 || (tA && lane < 48)) ST16(((u4){0u, 0u, 0u, 0u}), rs, lane == 63 ? (L_FL + b) * 128 : eo);
  if (lane < 16) *(unsigned*)(smem + LD_SY + lane * 4) = 0u;
  WAITL0();
  BAR();
  const char* hb = ws + (L_HB + (b & 31)) * 128;
  const unsigned h0 = sld(hb);
  if (tA) {
#pragma unroll
    for (int k = 0; k <= HARR; k++) BAR();
  }
  for (unsigned spins = 0; spins < SPIN_CAP; spins++) {
    if (sld(hb) != h0) break;
    __builtin_amdgcn_s_sleep(1);
  }
  WAITV(0);
  if (tA) BAR(); else lds_wait(sy(smem, Y_LN), 8u);
  if (lane == 0) {
    __hip_atomic_store(ctl(ws, L_HF + (b >> 2)) + (b & 3), 1u, RLX_AGENT);
    __hip_atomic_fetch_add(ctl(ws, L_DN + (b & 31)), 1u, RLX_AGENT);
  }
  STAMP(1);
  auto see_rows = [&](int nap) {
    for (unsigned spins = 0; spins < SPIN_CAP; spins++) {
      const u4 v = __builtin_amdgcn_raw_buffer_load_b128(rs, (L_HF + lane) * 128, 0, 16);
      if (__all((v[0] & v[1] & v[2] & v[3]) == 1u)) break;
      asm volatile("s_sleep 4" ::: "memory");
    }
    asm volatile("" ::: "memory");
  };
  if (tA) {
#pragma unroll
    for (int k = HARR + 2; k < 8; k++) BAR();
    see_rows(0);
    STAMP(2);
    BAR();
  }
  const gu32* sf = state_flag(ws, (xcd & 1) * 8 + (l >> 2), l & 3, lane);
  auto see_states = [&]() {
    unsigned fv;
    asm volatile("s_barrier\n\tglobal_load_dword %0, %1, off sc1\n\ts_barrier\n\ts_barrier\n\ts_barrier\n\ts_waitcnt vmcnt(0)"
                 : "=&v"(fv) : "v"(sf) : "memory");
    if (!__all(fv >= 1u)) wait_ge<2>(sf, 1u);
    STAMP(4);
    BAR();
  };
  if (!tA) {
    bump(smem, Y_GO, lane);
    if (tG1) __builtin_amdgcn_s_sleep(BNAP); else __builtin_amdgcn_s_sleep(100);
    see_rows(0);
    lds_put(sy(smem, Y_HOK), 1u);
    STAMP(2);
    if (tG1) {
#pragma nounroll
      for (int k = 0; k < 28; k++) BAR();
      see_states();
    } else {
      if (b < 252) {
        lds_idle(sy(smem, Y_SD), 4u);
        if (lane == 0) __hip_atomic_store(flag(ws, b, F_S), 1u, RLX_AGENT);
      }
      doze(flag(ws, blk_scan((xcd & 1) * 8 + (l >> 2), 0), F_S));
      __builtin_amdgcn_s_sleep(PFNAP);
      wait_ge<8>(sf, 1u);
      lds_put(sy(smem, Y_STOP), 1u);
      WAITL0();
    }
    return;
  }
  BAR();
  const f2 lm = *(const f2*)(smem + LD_LAM + L * 8);
  float pr = lm[0], pi = lm[1], r8 = 0.f, i8 = 0.f;
#pragma unroll
  for (int k = 0; k < 7; k++) {
    const float t = __builtin_fmaf(pr, pr, -(pi * pi)); pi = 2.f * pr * pi; pr = t;
    if (k == 2) { r8 = pr; i8 = pi; }
  }
  float r128 = pr, i128 = pi;
  asm volatile("" : "+v"(r8), "+v"(i8), "+v"(r128), "+v"(i128));
  BAR(); BAR(); BAR(); BAR(); BAR();
#define PIN2(a, b) asm volatile("s_barrier" : "+v"(a), "+v"(b) :: "memory")
  f2* agg = (f2*)(smem + LD_HT) + L;
  f2 ev[16];
#pragma unroll
  for (int k = 0; k < 16; k++) ev[k] = agg[k * 48];
  float xr = ev[0][0], xi = ev[0][1];
#pragma unroll
  for (int k = 1; k < 6; k++) cstep(xr, xi, r8, i8, ev[k][0], ev[k][1]);
  PIN2(xr, xi);
#pragma unroll
  for (int k = 6; k < 16; k++) cstep(xr, xi, r8, i8, ev[k][0], ev[k][1]);
  if (lane < 48) ST16(((u4){__float_as_uint(xr), 1u, __float_as_uint(xi), 1u}), rs, eo);
  BAR(); BAR(); BAR();
  u4 pv[15];
  auto ldp = [&](int k) { return __builtin_amdgcn_raw_buffer_load_b128(rs, k + rc >= 15 ? eo - (15 - k) * (576 * 16) : 0x80000000u, 0, 16); };
#pragma unroll
  for (int k = 0; k < 8; k++) pv[k] = ldp(k);
  BAR();
#pragma unroll
  for (int k = 8; k < 15; k++) pv[k] = ldp(k);
  unsigned z;
  asm volatile("s_barrier\n\ts_barrier\n\tv_mov_b32 %0, 0" : "=v"(z) :: "memory");
  for (unsigned spins = 0; spins < SPIN_CAP; spins++) {
    unsigned cnt = z;
#pragma unroll
    for (int k = 0; k < 15; k++) cnt += pv[k][1] & pv[k][3];
    if (__all(cnt == (unsigned)rc)) break;
#pragma unroll
    for (int k = 0; k < 15; k++) pv[k] = ldp(k);
    asm volatile("" ::: "memory");
  }
  xr = 0.f; xi = 0.f;
  PIN2(xr, xi);
#pragma unroll
  for (int k = 0; k < 15; k++) cstep(xr, xi, r128, i128, __uint_as_float(pv[k][0]), __uint_as_float(pv[k][2]));
  PIN2(xr, xi);
#pragma unroll
  for (int k = 0; k < 16; k++) {
    const f2 e = ev[k];
    ev[k] = (f2){xr, xi};
    if (k < 15) cstep(xr, xi, r8, i8, e[0], e[1]);
  }
  PIN2(xr, xi);
  if (lane < 48) {
#pragma unroll
    for (int k = 0; k < 16; k++) agg[k * 48] = ev[k];
  }
  WAITL0();
  STAMP(3);
#pragma unroll
  for (int k = 16; k < 24; k++) BAR();
  if (lane == 0) __hip_atomic_store(flag(ws, b, F_S), 1u, RLX_AGENT);
  BAR(); BAR(); BAR(); BAR();
  see_states();
}

__device__ __forceinline__ void extra(const Args& A, char* smem, int lane) {
  char* ws = A.ws;
  const int b = blockIdx.x, xcd = b & 7, l = b >> 3;
  const bool tA = l < 24;
  const int ch = tA ? ((xcd & 1) * 6 + (l >> 2)) * 48 + chan_of(lane < 48 ? lane : 47) : (((l + 9) * 8 + xcd) % 9) * 64 + lane;
  unsigned lrb = A.lre[ch], lib = A.lim[ch], dtb = A.logdt[ch];
  if (b == 255) {
    const rsrc_t rs = __builtin_amdgcn_make_buffer_rsrc(ws, 0, WS_END, 0x00020000);
#pragma unroll
    for (int i = 0; i < 2; i++) ST16(((u4){0u, 0u, 0u, 0u}), rs, (i ? L_DN + (lane & 31) : L_HF + lane) * 128);
  }
  BAR();
#define LOGB(k)
  if (b == 255) {
    WAITV(0);
    unsigned beat = 0;
    for (unsigned spins = 0; spins < SPIN_CAP; spins++) {
#pragma unroll
      for (int k = 0; k < 4; k++) {
        ++beat;
        if (lane < 32) __hip_atomic_store(ctl(ws, L_HB + lane), beat, RLX_AGENT);
        __builtin_amdgcn_s_sleep(4);
      }
      const unsigned v = __hip_atomic_load(ctl(ws, L_DN + (lane & 31)), RLX_AGENT);
      if (__all(v >= 8u)) break;
    }
  }
  asm volatile("" : "+v"(lrb), "+v"(lib), "+v"(dtb));
  const float lr = bflo(lrb), li = bflo(lib);
  float dt = rb(expf(bflo(dtb)));
  float er = expf(dt * lr);
  if (tA) { asm volatile("s_barrier" : "+v"(dt), "+v"(er) :: "memory"); LOGB(0); }
  float sn, cs;
  sincosf(dt * li, &sn, &cs);
  if (tA) { asm volatile("s_barrier" : "+v"(sn), "+v"(cs) :: "memory"); LOGB(1); }
  const float ar = er * cs, ai = er * sn;
  const float nr = ar - 1.f, id = 2.f / (lr * lr + li * li);
  *(f2*)(smem + LD_LAM + lane * 8) = tA ? (f2){ar, ai} : (f2){(nr * lr + ai * li) * id, (ai * lr - nr * li) * id};
  if (!tA) {
    bump(smem, Y_PAR, lane);
    WAITL0();
    return;
  }
  WAITL0();
#pragma nounroll
  for (int k = 2; k < 9; k++) { BAR(); LOGB(k); }
  const char* p = spf_base(A.w2, SP0);
#pragma nounroll
  for (int i = 0; i < SP_X; i++) {
    BAR(); LOGB(9 + i);
    spf(p);
    p += 6 * 4096;
  }
  WAITL0();
}

__global__ __launch_bounds__(512) void k_fused(Args A) {
  extern __shared__ __attribute__((aligned(16))) char smem[];
  const int lane = threadIdx.x & 63, w = __builtin_amdgcn_readfirstlane(threadIdx.x >> 6);
  if (w < 4) consumer(A, smem, lane, w);
  else if (w < 6) producer(A, smem, lane, w - 4);
  else if (w == 6) helper(A, smem, lane);
  else extra(A, smem, lane);
}

__global__ void k_spin(char* ws, int wave, int idx, int lmin, int lmax, int op) {
  if (threadIdx.x != 0) return;
  const unsigned long long* d = (const unsigned long long*)(ws + OFF_DBG);
  unsigned long long t0 = ~0ull;
  for (int b = 0; b < 256; b++) { unsigned long long t = d[(b * 8) * 16]; t0 = t < t0 ? t : t0; }
  unsigned long long mn = ~0ull, mx = 0, sum = 0; int n = 0;
  for (int b = 0; b < 256; b++) { int l = b >> 3; if (l < lmin || l > lmax) continue;
    unsigned long long t = d[(b * 8 + wave) * 16 + (idx < 0 ? 0 : idx)] - t0; mn = t < mn ? t : mn; mx = t > mx ? t : mx; sum += t; n++; }
  unsigned long long x = op == 0 ? mn : op == 1 ? mx : sum / (n ? n : 1);
  if (idx < 0) x = 0;
  if (x > 20000) x = 20000;
  const unsigned long long tgt = t0 + 15000 + 2 * x;
  while (__builtin_amdgcn_s_memrealtime() < tgt) __builtin_amdgcn_s_sleep(1);
}
extern "C" void kernel_launch(void* const* d_in, const int* in_sizes, int n_in, void* d_out, int out_size,
                              void* d_ws, size_t ws_size, hipStream_t stream) {
  Args a;
  a.x = (const u16*)d_in[0]; a.lre = (const u16*)d_in[1]; a.lim = (const u16*)d_in[2];
  a.bre = (const u16*)d_in[3]; a.bim = (const u16*)d_in[4]; a.cre = (const u16*)d_in[5]; a.cim = (const u16*)d_in[6];
  a.logdt = (const u16*)d_in[7]; a.dskip = (const u16*)d_in[8];
  a.w1 = (const u16*)d_in[10]; a.b1 = (const u16*)d_in[11]; a.w2 = (const u16*)d_in[12]; a.b2 = (const u16*)d_in[13];
  a.ws = (char*)d_ws; a.out = (float*)d_out;
  (void)hipFuncSetAttribute((const void*)k_fused, hipFuncAttributeMaxDynamicSharedMemorySize, LDS_BYTES);
  k_fused<<<256, 512, LDS_BYTES, stream>>>(a);
  k_spin<<<1, 64, 0, stream>>>(a.ws, 0,-1,0,31,0);
}
```

```cpp
#include <hip/hip_runtime.h>
#include <stdint.h>

typedef unsigned short u16;
typedef short s4 __attribute__((ext_vector_type(4)));
typedef __bf16 b2 __attribute__((ext_vector_type(2)));
typedef __bf16 b8 __attribute__((ext_vector_type(8)));
typedef float f2 __attribute__((ext_vector_type(2)));
typedef float f4 __attribute__((ext_vector_type(4)));
typedef unsigned u2 __attribute__((ext_vector_type(2)));
typedef unsigned u4 __attribute__((ext_vector_type(4)));
#define GAS __attribute__((address_space(1)))
#define LAS __attribute__((address_space(3)))
typedef GAS unsigned gu32;
typedef __amdgpu_buffer_rsrc_t rsrc_t;
#define RLX_AGENT __ATOMIC_RELAXED, __HIP_MEMORY_SCOPE_AGENT

constexpr int D_ = 576, F_ = 4608;

constexpr int HREP = 4;
constexpr int L_HB = 0, L_FL = 64, L_IN = 320, L_DN = 832, L_HF = 896;
constexpr int F_S = 1;
constexpr unsigned OFF_E    = 196608;
constexpr unsigned OFF_STAT = OFF_E + 16 * 576 * 16;
constexpr unsigned OFF_HF   = 1u << 20;
constexpr unsigned OFF_PF   = OFF_HF + 64 * 36 * 1024;
constexpr unsigned OFF_HS   = OFF_PF + 180 * 36 * 1024;
constexpr unsigned OFF_SLAB = OFF_HS + 1152 * 2048 * 2;
constexpr unsigned WS_END   = OFF_SLAB + 64 * 16 * 9216;

constexpr int SLOT = 34816, WOFS = 18432;
constexpr int LD_HT = 4 * SLOT;
constexpr int LD_ST = LD_HT + 9216;
constexpr int LD_LAM = LD_ST + 1024;
constexpr int LD_BS = LD_LAM + 512;
constexpr int LD_SCR = LD_BS + 512;
constexpr int LD_SY = LD_SCR + 256;
constexpr int LDS_BYTES = LD_SY + 64;
enum { Y_LN = 0, Y_GO, Y_SD, Y_HOK, Y_PAR, Y_STOP };

__device__ __forceinline__ float bflo(unsigned v) { return __uint_as_float(v << 16); }
__device__ __forceinline__ float bfhi(unsigned v) { return __uint_as_float(v & 0xffff0000u); }
__device__ __forceinline__ unsigned pk2(float a, float b) {
  f2 z = {a, b}; b2 r = __builtin_convertvector(z, b2); return __builtin_bit_cast(unsigned, r);
}
__device__ __forceinline__ float rb(float a) { return bflo(pk2(a, 0.f)); }
template <int CTRL> __device__ __forceinline__ float dpp(float e) {
  return __builtin_bit_cast(float, __builtin_amdgcn_update_dpp(0, __builtin_bit_cast(int, e), CTRL, 0xF, 0xF, true));
}
__device__ __forceinline__ float rdl(float v, int l) {
  return __builtin_bit_cast(float, __builtin_amdgcn_readlane(__builtin_bit_cast(int, v), l));
}
__device__ __forceinline__ float wave_sum(float v) {
  v += dpp<0xB1>(v); v += dpp<0x4E>(v); v += dpp<0x141>(v); v += dpp<0x140>(v);
  return (rdl(v, 0) + rdl(v, 16)) + (rdl(v, 32) + rdl(v, 48));
}
__device__ __forceinline__ void glds16(const char* g, char* l) {
  __builtin_amdgcn_global_load_lds((const GAS void*)g, (LAS void*)l, 16, 0, 0);
}
template <int IMM, int AUX>
__device__ __forceinline__ void blds(rsrc_t rs, char* l, unsigned voff, unsigned soff) {
  __builtin_amdgcn_raw_ptr_buffer_load_lds(rs, (LAS void*)l, 16, voff, soff, IMM, AUX);
}
template <int AUX>
__device__ __forceinline__ void blds4(int q, rsrc_t rs, char* l, unsigned voff, unsigned soff) {
  if (q == 0) blds<0, AUX>(rs, l, voff, soff);
  else if (q == 1) blds<1024, AUX>(rs, l, voff, soff);
  else if (q == 2) blds<2048, AUX>(rs, l, voff, soff);
  else blds<3072, AUX>(rs, l, voff, soff);
}
__device__ __forceinline__ void touch(const char* g, char* l) {
  __builtin_amdgcn_global_load_lds((const GAS void*)g, (LAS void*)l, 4, 0, 0);
}
__device__ __forceinline__ b8 ld_frag(const char* p) { return *(const LAS b8*)(LAS const char*)p; }
__device__ __forceinline__ b8 ld_tr(const char* p0, const char* p1) {
  s4 a = __builtin_amdgcn_ds_read_tr16_b64_v4i16((LAS s4*)(LAS char*)p0);
  s4 b = __builtin_amdgcn_ds_read_tr16_b64_v4i16((LAS s4*)(LAS char*)p1);
  return __builtin_bit_cast(b8, __builtin_shufflevector(a, b, 0, 1, 2, 3, 4, 5, 6, 7));
}
#define ST16(v, rs, off) __builtin_amdgcn_raw_buffer_store_b128(v, rs, off, 0, 16)
#define MFMA(a, b, c) __builtin_amdgcn_mfma_f32_16x16x32_bf16(a, b, c, 0, 0, 0)
#define BAR() asm volatile("s_barrier" ::: "memory")
#define WAITV(n) asm volatile("s_waitcnt vmcnt(%0)" ::"n"(n) : "memory")
#define WAITL0() asm volatile("s_waitcnt lgkmcnt(0)" ::: "memory")
#define SCHED() __builtin_amdgcn_sched_barrier(0)
#define SPIN_CAP (1u << 18)

__device__ __forceinline__ unsigned lds_addr(const char* p) { return (unsigned)(size_t)(LAS const char*)p; }
__device__ __forceinline__ unsigned lds_get(unsigned a) {
  unsigned v;
  asm volatile("ds_read_b32 %0, %1\n\ts_waitcnt lgkmcnt(0)" : "=v"(v) : "v"(a) : "memory");
  return v;
}
__device__ __forceinline__ void lds_put(unsigned a, unsigned v) { asm volatile("ds_write_b32 %0, %1" :: "v"(a), "v"(v) : "memory"); }
__device__ __forceinline__ void lds_wait(unsigned a, unsigned need) {
#pragma nounroll
  for (unsigned spins = 0; spins < (SPIN_CAP << 4); spins++)
    if (__all(lds_get(a) >= need)) break;
}
__device__ __forceinline__ void lds_idle(unsigned a, unsigned need) {
#pragma nounroll
  for (unsigned spins = 0; spins < (SPIN_CAP << 4); spins++) {
    if (__all(lds_get(a) >= need)) break;
    __builtin_amdgcn_s_sleep(2);
  }
}
__device__ __forceinline__ unsigned sy(char* smem, int y) { return lds_addr(smem + LD_SY + y * 4); }
__device__ __forceinline__ void bump(char* smem, int y, int lane) {
  if (lane == 0) asm volatile("s_waitcnt lgkmcnt(0)\n\tds_add_u32 %0, %1" :: "v"(sy(smem, y)), "v"(1u) : "memory");
}

__device__ __forceinline__ gu32* ctl(char* ws, int line) { return (gu32*)ws + line * 32; }
__device__ __forceinline__ gu32* flag(char* ws, int blk, int f) { return ctl(ws, L_FL + blk) + f; }
__device__ __forceinline__ int f_rot(const u16* w1, int j) { return (2 - (int)((size_t)w1 >> 8) - 9 * j) & 3; }
__device__ __forceinline__ int f_tile(int j, int rot, int i) { return j * 9 + (rot + i >= 9 ? rot + i - 9 : rot + i); }
__device__ __forceinline__ int s_tile(const u16* w2, int xcd, int q) {
  const int slow = (1 - (int)((size_t)w2 >> 8)) & 3;
  const int t = q >= 6 ? slow + (q - 6) * 4 : (q / 3) * 4 + ((slow + 1 + q % 3) & 3);
  return (xcd & 1) * 8 + t;
}
__device__ __forceinline__ int chan_of(int L) { return (L & ~15) + 4 * ((L & 7) >> 1) + 2 * ((L >> 3) & 1) + (L & 1); }
__device__ __forceinline__ void cstep(float& xr, float& xi, float lr, float li, float er, float ei) {
  float tr, ti;
  asm("v_fma_f32 %0, %4, %2, %6\n\tv_fma_f32 %1, %4, %3, %7\n\tv_fma_f32 %0, -%5, %3, %0\n\tv_fma_f32 %1, %5, %2, %1"
      : "=&v"(tr), "=&v"(ti) : "v"(xr), "v"(xi), "v"(lr), "v"(li), "v"(er), "v"(ei));
  xr = tr; xi = ti;
}
__device__ __forceinline__ int blk_scan(int rc, int cg) { return ((cg % 6) * 4 + (rc & 3)) * 8 + (rc >> 2) * 2 + cg / 6; }
__device__ __forceinline__ const gu32* state_flag(char* ws, int st, int dq, int lane) {
  const int k = lane < 21 ? lane : 0;
  return flag(ws, k < 12 ? blk_scan(st, k) : 216 + dq * 9 + (k - 12), F_S);
}
template <int NAP>
__device__ __forceinline__ void wait_ge(const gu32* p, unsigned need) {
#pragma nounroll
  for (unsigned spins = 0; spins < SPIN_CAP; spins++) {
    const unsigned v = __hip_atomic_load(p, RLX_AGENT);
    if (__all(v >= need)) break;
    __builtin_amdgcn_s_sleep(NAP);
  }
  __builtin_amdgcn_fence(__ATOMIC_ACQUIRE, "wavefront");
}

__device__ __forceinline__ unsigned sld(const char* p) {
  unsigned v;
  asm volatile("s_load_dword %0, %1, 0x0 glc\n\ts_waitcnt lgkmcnt(0)" : "=s"(v) : "s"(p) : "memory");
  return v;
}

__device__ __forceinline__ void spf(const char* p) {
  asm volatile("s_load_dword s90, %0, 0x0\n\ts_load_dword s90, %0, 0x40\n\ts_load_dword s90, %0, 0x80\n\ts_load_dword s90, %0, 0xc0\n\t"
               "s_load_dword s90, %0, 0x100\n\ts_load_dword s90, %0, 0x140\n\ts_load_dword s90, %0, 0x180\n\ts_load_dword s90, %0, 0x1c0"
               :: "s"(p) : "s90", "memory");
}
constexpr int PF_B = 7;
constexpr int SP_X = 44, SP_H = 28;
constexpr int TL_ROWS = 27 * 2 * 4, SP0 = 1152 - 6 * (SP_X + SP_H), PF_W2 = (SP0 - TL_ROWS) / 4;
__device__ __forceinline__ const char* w2_row(const u16* w2, int rot, int r) {
  const int x = blockIdx.x & 7, q = r + rot * 128;
  return (const char*)w2 + (size_t)((x >> 1) * 1152 + (q >= 1152 ? q - 1152 : q)) * 4096 + (x & 1) * 2048;
}
struct Spf {
  const u16* w2; int rot, r, step;
  __device__ __forceinline__ void go() { spf(w2_row(w2, rot, r) + ((blockIdx.x >> 3) & 3) * 512); r += step; }
};

typedef unsigned u8v __attribute__((ext_vector_type(8)));
template <int NAP>
__device__ __forceinline__ void swait6(const gu32* p) {
#pragma nounroll
  for (unsigned spins = 0; spins < SPIN_CAP; spins++) {
    u8v v;
    asm volatile("s_load_dwordx8 %0, %1, 0x0 glc\n\ts_waitcnt lgkmcnt(0)" : "=&s"(v) : "s"(p) : "memory");
    if (v[0] & v[1] & v[2] & v[3] & v[4] & v[5]) break;
    __builtin_amdgcn_s_sleep(NAP);
  }
}
__device__ __forceinline__ void doze(const gu32* f) {
#pragma nounroll
  for (unsigned spins = 0; spins < SPIN_CAP && sld((const char*)f) == 0u; spins++) __builtin_amdgcn_s_sleep(8);
}

__device__ __forceinline__ u4 gelu8(f4 a, f4 b) {
  const float c0 = -2.f * 0.7978845608028654f * 1.4426950408889634f;
  const f2 k0 = {c0, c0}, k1 = {c0 * 0.044715f, c0 * 0.044715f}, one = {1.f, 1.f};
  f2 u[4] = {{a[0], a[1]}, {a[2], a[3]}, {b[0], b[1]}, {b[2], b[3]}}, t[4];
#pragma unroll
  for (int i = 0; i < 4; i++) t[i] = u[i] * u[i];
#pragma unroll
  for (int i = 0; i < 4; i++) t[i] = __builtin_elementwise_fma(t[i], k1, k0);
#pragma unroll
  for (int i = 0; i < 4; i++) t[i] = t[i] * u[i];
  SCHED();
#pragma unroll
  for (int i = 0; i < 4; i++) t[i] = (f2){__builtin_amdgcn_exp2f(t[i][0]), __builtin_amdgcn_exp2f(t[i][1])};
  SCHED();
#pragma unroll
  for (int i = 0; i < 4; i++) t[i] = t[i] + one;
  SCHED();
#pragma unroll
  for (int i = 0; i < 4; i++) t[i] = (f2){__builtin_amdgcn_rcpf(t[i][0]), __builtin_amdgcn_rcpf(t[i][1])};
  SCHED();
  u4 o;
#pragma unroll
  for (int i = 0; i < 4; i++) t[i] = t[i] * u[i];
#pragma unroll
  for (int i = 0; i < 4; i++) o[i] = pk2(t[i][0], t[i][1]);
  return o;
}

struct Args {
  const u16 *x, *lre, *lim, *bre, *bim, *cre, *cim, *logdt, *dskip, *w1, *b1, *w2, *b2;
  char* ws; float* out;
};

template <int NP, bool WFIRST>
struct Tile {
  f4 acc[2][NP];
  b8 P0[NP], W0[2], P1[NP], W1[2];
  int wofs[2], pofs;
  __device__ __forceinline__ void rd(const char* sl, int ks, b8* P, b8* W) {
#pragma unroll
    for (int a = 0; a < 2; a++) W[a] = ld_tr(sl + wofs[a] + ks * 8192, sl + wofs[a] + ks * 8192 + 4096);
#pragma unroll
    for (int i = 0; i < NP; i++) P[i] = ld_frag(sl + pofs + ks * 9216 + i * 1024);
  }
  __device__ __forceinline__ void mm(const b8* P, const b8* W) {
#pragma unroll
    for (int i = 0; i < NP; i++)
#pragma unroll
      for (int a0 = 0; a0 < 2; a0++) {
        const int a = a0 ^ (i & 1);
        acc[a][i] = WFIRST ? MFMA(W[a], P[i], acc[a][i]) : MFMA(P[i], W[a], acc[a][i]);
      }
#pragma unroll
    for (int r = 0; r < 4 + NP; r++) {
      __builtin_amdgcn_sched_group_barrier(0x8, 1, 0);
      __builtin_amdgcn_sched_group_barrier(0x100, 1, 0);
    }
    __builtin_amdgcn_sched_group_barrier(0x8, 2 * NP, 0);
  }
  template <class G>
  __device__ __forceinline__ void half(const char* sl, int ks, const b8* P, const b8* W, b8* Pn, b8* Wn, int t, G side) {
#pragma unroll
    for (int i = 0; i < NP; i++) {
#pragma unroll
      for (int a0 = 0; a0 < 2; a0++) {
        const int a = a0 ^ (i & 1);
        acc[a][i] = WFIRST ? MFMA(W[a], P[i], acc[a][i]) : MFMA(P[i], W[a], acc[a][i]);
      }
      if (i < 2) Wn[i] = ld_tr(sl + wofs[i] + ks * 8192, sl + wofs[i] + ks * 8192 + 4096);
      else if (2 * i - 4 < NP) {
        Pn[2 * i - 4] = ld_frag(sl + pofs + ks * 9216 + (2 * i - 4) * 1024);
        Pn[2 * i - 3] = ld_frag(sl + pofs + ks * 9216 + (2 * i - 3) * 1024);
      }
      __builtin_amdgcn_sched_group_barrier(0x8, 1, 0); __builtin_amdgcn_sched_group_barrier(0x100, 1, 0);
      __builtin_amdgcn_sched_group_barrier(0x8, 1, 0); __builtin_amdgcn_sched_group_barrier(0x100, 1, 0);
      SCHED();
      side(t, i);
      SCHED();
    }
  }
  __device__ __forceinline__ void first(const char* smem) { rd(smem, 0, P0, W0); }
  template <bool DRAIN = false, class F>
  __device__ __forceinline__ void step(const char* smem, int u, F mid) {
    SCHED();
    rd(smem + u * SLOT, 1, P1, W1);
    mm(P0, W0);
    SCHED();
    if (DRAIN) WAITV(0);
    WAITL0();
    BAR();
    mid();
    SCHED();
    rd(smem + ((u + 1) & 3) * SLOT, 0, P0, W0);
    mm(P1, W1);
    SCHED();
  }
  template <class G>
  __device__ __forceinline__ void step_side(const char* smem, int u, G side) {
    SCHED();
    half(smem + u * SLOT, 1, P0, W0, P1, W1, 2 * u, side);
    WAITL0();
    BAR();
    SCHED();
    half(smem + ((u + 1) & 3) * SLOT, 0, P1, W1, P0, W0, 2 * u + 1, side);
  }
};
#define DPP_SWAP " quad_perm:[1,0,3,2] row_mask:0xf bank_mask:0xf"

__device__ __forceinline__ void consumer(const Args& A, char* smem, int lane, int w) {
  char* ws = A.ws;
  const int b = blockIdx.x, xcd = b & 7, l = b >> 3;
  const int g = lane >> 4, c = lane & 15, q = c >> 2, p = lane & 3;
  const rsrc_t rs = __builtin_amdgcn_make_buffer_rsrc(ws, 0, WS_END, 0x00020000);
  const bool tA = l < 24, tG1 = l < 27;
  const int t1 = xcd * 27 + (tG1 ? l : 0), ft = t1 / 6, mt = t1 % 6;
  u2 bias1[2];
  const int lg = (xcd >> 1) * 64 + l * 2 + (xcd & 1);
  {
    const int s0 = (lg >> 2) * 32 + (lg & 3) * 4 + (w & 1) * 2 + (w >> 1) * 16;
    unsigned xr[2][9];
#pragma unroll
    for (int r = 0; r < 2; r++)
#pragma unroll
      for (int i = 0; i < 9; i++) xr[r][i] = A.x[(s0 + r) * D_ + i * 64 + lane];
#pragma unroll
    for (int a = 0; a < 2; a++) bias1[a] = *(const u2*)(A.b1 + ft * 128 + (2 * w + a) * 16 + 4 * g);
    unsigned sh16;
    asm volatile("s_waitcnt lgkmcnt(0)\n\ts_barrier\n\tv_mov_b32 %0, 16" : "=v"(sh16) :: "memory");
    f2 v[9], a = {0.f, 0.f};
#pragma unroll
    for (int i = 0; i < 9; i++) { v[i] = (f2){__uint_as_float(xr[0][i] << sh16), __uint_as_float(xr[1][i] << sh16)}; a += v[i]; }
    const f2 m = (f2){wave_sum(a[0]), wave_sum(a[1])} * (1.f / D_);
    f2 qq = {0.f, 0.f};
#pragma unroll
    for (int i = 0; i < 9; i++) { const f2 d = v[i] - m; qq += d * d; }
    const f2 var = (f2){wave_sum(qq[0]), wave_sum(qq[1])} * (1.f / D_);
    f2 mb, r1, sc, sh;
#pragma unroll
    for (int r = 0; r < 2; r++) {
      mb[r] = rb(m[r]);
      r1[r] = __builtin_amdgcn_rcpf(rb(__builtin_amdgcn_sqrtf(rb(rb(var[r]) + 1e-5f))));
      const float mb2 = rb((m[r] - mb[r]) * r1[r]);
      const float r2 = __builtin_amdgcn_rcpf(rb(__builtin_amdgcn_sqrtf(rb(rb(var[r] * r1[r] * r1[r]) + 1e-5f))));
      sc[r] = r1[r] * r2; sh[r] = -mb2 * r2;
    }
#pragma unroll
    for (int i = 0; i < 9; i++) {
      const f2 h = (v[i] - mb) * sc + sh;
      *(unsigned*)(smem + LD_HT + (i * 64 + lane) * 16 + w * 4) = pk2(h[0], h[1]);
    }
    if (lane == 0)
      ST16(((u4){__float_as_uint(mb[0]), __float_as_uint(r1[0]), __float_as_uint(mb[1]), __float_as_uint(r1[1])}), rs,
           OFF_STAT + s0 * 8);
  }
  bump(smem, Y_LN, lane);
  lds_wait(sy(smem, Y_LN), 4u);
#pragma unroll
  for (int r = 0; r < 3; r++) {
    const int d = r * 256 + w * 64 + lane;
    if (r < 2 || w == 0)
      ST16(*(const u4*)(smem + LD_HT + d * 16), rs, OFF_HF + ((lg >> 2) * 36 + (d >> 4)) * 1024 + ((lg & 3) * 16 + (d & 15)) * 16);
  }
  f4 bu[2][6];
  const int rc = (xcd >> 1) * 4 + (l & 3), cg = (xcd & 1) * 6 + (l >> 2);
  if (tA) {
    f4 bs[2] = {{0.f, 0.f, 0.f, 0.f}, {0.f, 0.f, 0.f, 0.f}};
#pragma unroll
    for (int i = 0; i < 2; i++)
#pragma unroll
      for (int n = 0; n < 6; n++) bu[i][n] = (f4){0.f, 0.f, 0.f, 0.f};
    const u4 one4 = {0x3f803f80u, 0x3f803f80u, 0x3f803f80u, 0x3f803f80u};
    const b8 ones = __builtin_bit_cast(b8, one4);
    int fo[2];
#pragma unroll
    for (int ks = 0; ks < 2; ks++) fo[ks] = c * 128 + (((ks * 4 + g) ^ (c >> 1)) << 4);
    b8 X0[2], B0[8], X1[2], B1[8];
    auto rd = [&](const char* sl, int ks, b8* X, b8* B) {
#pragma unroll
      for (int i = 0; i < 2; i++) X[i] = ld_frag(sl + fo[ks] + w * 4096 + i * 2048);
#pragma unroll
      for (int n = 0; n < 6; n++) B[n] = ld_frag(sl + fo[ks] + 16384 + n * 2048);
      B[6] = ld_frag(sl + fo[ks] + 16384 + w * 2048);
      B[7] = ld_frag(sl + fo[ks] + 16384 + (4 + (w & 1)) * 2048);
    };
    auto mm = [&](const b8* X, const b8* B) {
#pragma unroll
      for (int n = 0; n < 6; n++)
#pragma unroll
        for (int i0 = 0; i0 < 2; i0++) { const int i = i0 ^ (n & 1); bu[i][n] = MFMA(X[i], B[n], bu[i][n]); }
      bs[0] = MFMA(ones, B[6], bs[0]);
      bs[1] = MFMA(ones, B[7], bs[1]);
#pragma unroll
      for (int r = 0; r < 11; r++) {
        __builtin_amdgcn_sched_group_barrier(0x8, 1, 0);
        __builtin_amdgcn_sched_group_barrier(0x100, 1, 0);
      }
      __builtin_amdgcn_sched_group_barrier(0x8, 14, 0);
    };
    BAR();
    rd(smem + 3 * SLOT, 0, X0, B0);
#pragma unroll
    for (int s = 0; s < 9; s++) {
      SCHED();
      rd(smem + ((s + 3) & 3) * SLOT, 1, X1, B1);
      mm(X0, B0);
      SCHED();
      if (s < 8) {
        if (s == 0) WAITV(0);
        WAITL0();
        BAR();
        SCHED();
        rd(smem + (s & 3) * SLOT, 0, X0, B0);
      }
      mm(X1, B1);
      SCHED();
    }
    if (lane < 16) {
      *(float*)(smem + LD_BS + (w * 16 + c) * 4) = bs[0][0];
      *(float*)(smem + LD_BS + ((4 + (w & 1)) * 16 + c) * 4) = bs[1][0];
    }
  } else {
    WAITV(0);
    bump(smem, Y_LN, lane);
    const int fb = (l - 27) * 8 + xcd, kg = fb % 9, du = fb / 9;
    if (!tG1 && fb < 36) {
      lds_idle(sy(smem, (lane & 1) ? Y_GO : Y_PAR), 1u);
      u4 cr[3][2], ci[3][2];
#pragma unroll
      for (int r = 0; r < 3; r++) {
        const int o = ((du * 9 + (w + 4 * r < 9 ? w + 4 * r : w)) * 16 + c) * D_ + kg * 64 + g * 8;
#pragma unroll
        for (int h = 0; h < 2; h++) { cr[r][h] = *(const u4*)(A.cre + o + h * 32); ci[r][h] = *(const u4*)(A.cim + o + h * 32); }
      }
#pragma unroll
      for (int h = 0; h < 2; h++) {
        f4 ff[4];
#pragma unroll
        for (int k = 0; k < 4; k++) ff[k] = *(const f4*)(smem + LD_LAM + (h * 32 + g * 8 + 2 * k) * 8);
#pragma unroll
        for (int r = 0; r < 3; r++) {
          if (w + 4 * r < 9) {
#pragma unroll
            for (int e = 0; e < 2; e++) {
              u4 o;
#pragma unroll
              for (int t = 0; t < 2; t++) {
                const unsigned a = cr[r][h][2 * e + t], bq = ci[r][h][2 * e + t];
                const f4 f = ff[2 * e + t];
                o[t * 2] = pk2(bflo(a) * f[0] - bflo(bq) * f[1], -(bflo(a) * f[1] + bflo(bq) * f[0]));
                o[t * 2 + 1] = pk2(bfhi(a) * f[2] - bfhi(bq) * f[3], -(bfhi(a) * f[3] + bfhi(bq) * f[2]));
              }
              ST16(o, rs, OFF_PF + ((144 + kg * 4 + h * 2 + (g >> 1)) * 36 + du * 9 + w + 4 * r) * 1024 + (((g & 1) * 2 + e) * 16 + c) * 16);
            }
          }
        }
      }
      WAITV(0);
      bump(smem, Y_SD, lane);
    }
  }
  const int wW[2] = {WOFS + (4 * g + q) * 256 + (((2 * w) ^ (4 * (g & 1) + q)) * 32) + p * 8,
                     WOFS + (4 * g + q) * 256 + (((2 * w + 1) ^ (4 * (g & 1) + q)) * 32) + p * 8};
  if (tG1) {
    Tile<6, true> T;
    T.wofs[0] = wW[0]; T.wofs[1] = wW[1]; T.pofs = lane * 16;
#pragma unroll
    for (int a = 0; a < 2; a++) {
      const u2 bv = bias1[a];
#pragma unroll
      for (int i = 0; i < 6; i++) T.acc[a][i] = (f4){bflo(bv[0]), bfhi(bv[0]), bflo(bv[1]), bfhi(bv[1])};
    }
    WAITL0();
    BAR();
    T.first(smem);
    float* agg = (float*)(smem + LD_HT) + (w * 4 + g) * 96 + c;
    auto nop = []() {};
    float ar[6], ai[6], hv[8][6];
    auto lam = [&]() {
      const float sg = (c & 1) ? 1.f : -1.f;
#pragma unroll
      for (int n = 0; n < 6; n++) {
        const f2 lm = *(const f2*)(smem + LD_LAM + (n * 8 + (c >> 1)) * 8);
        ar[n] = lm[0]; ai[n] = lm[1] * sg;
      }
    };
    auto plain = [&](int rounds) {
#pragma nounroll
      for (int r = 0; r < rounds; r++) {
        T.step(smem, 0, nop);
        T.template step<true>(smem, 1, nop);
        T.step(smem, 2, nop); T.step(smem, 3, nop);
      }
    };
    if (tA) {
      float bsum[6], e[6];
      f4 stt[4];
#pragma unroll
      for (int t = 0; t < 4; t++) stt[t] = *(const f4*)(smem + LD_ST + (w * 32 + g * 8 + 2 * t) * 8);
#pragma unroll
      for (int n = 0; n < 6; n++) { bsum[n] = *(const float*)(smem + LD_BS + (n * 16 + c) * 4); e[n] = 0.f; }
      lam();
      auto p1 = [&](int t, int n) {
        float en;
        hv[t][n] = bu[t >> 2][n][t & 3];
        asm volatile("v_fma_f32 %0, -%2, %4, %0\n\tv_mul_f32 %0, %0, %3\n\tv_fma_f32 %1, %5, %7, %0\n\tv_fmac_f32_dpp %1, %7, %6" DPP_SWAP
                     : "+v"(hv[t][n]), "=&v"(en)
                     : "v"(stt[t >> 1][(t & 1) * 2]), "v"(stt[t >> 1][(t & 1) * 2 + 1]), "v"(bsum[n]), "v"(ar[n]), "v"(ai[n]), "v"(e[n]));
        e[n] = en;
      };
      T.step_side(smem, 0, p1); T.step_side(smem, 1, p1); T.step_side(smem, 2, p1); T.step_side(smem, 3, p1);
#pragma unroll
      for (int n = 0; n < 6; n++) agg[n * 16] = e[n];
    }
    plain(tA ? 3 : 4);
    if (tA) {
      float e[6];
      lam();
#pragma unroll
      for (int n = 0; n < 6; n++) e[n] = agg[n * 16];
      const unsigned hb = OFF_HS + rc * (36 * 8192) + cg * 6 * 4096 + (2 * w + (g >> 1)) * 512 + (g & 1) * 256 +
                          ((q ^ (2 * (g & 1))) * 64) + (c & 3) * 16;
      auto p2 = [&](int h, int i) {
        if (h < 4) {
          const int t = 2 * h + i / 3;
#pragma unroll
          for (int n = (i % 3) * 2; n < (i % 3) * 2 + 2; n++)
            asm volatile("v_fma_f32 %0, %1, %3, %0\n\tv_fmac_f32_dpp %0, %3, %2" DPP_SWAP
                         : "+v"(hv[t][n]) : "v"(ar[n]), "v"(ai[n]), "v"(t ? hv[t ? t - 1 : 0][n] : e[n]));
        } else if (h < 7 && i % 3 == 0) {
          const int n = (h - 4) * 2 + i / 3;
          ST16(((u4){pk2(hv[0][n], hv[1][n]), pk2(hv[2][n], hv[3][n]), pk2(hv[4][n], hv[5][n]), pk2(hv[6][n], hv[7][n])}),
               rs, hb + n * 4096);
        }
      };
      T.step_side(smem, 0, p2); T.step_side(smem, 1, p2); T.step_side(smem, 2, p2); T.step_side(smem, 3, p2);
    }
    plain(tA ? 3 : 4);
    const unsigned ub = OFF_PF + ((ft * 4 + w) * 36 + mt * 6) * 1024 + lane * 16;
#pragma unroll
    for (int i = 0; i < 6; i++)
      ST16(gelu8(T.acc[0][i], T.acc[1][i]), rs, ub + i * 1024);
  }
  const int j = xcd >> 1, st = s_tile(A.w2, xcd, l >> 2), dq = l & 3, tile = st * 4 + dq;
  Tile<9, false> T;
  T.pofs = lane * 16;
#pragma unroll
  for (int a = 0; a < 2; a++) {
    T.wofs[a] = WOFS + (2 * w + a) * 512 + (p >> 1) * 256 + ((g ^ (p & 2)) * 64) + q * 16 + (p & 1) * 8;
#pragma unroll
    for (int i = 0; i < 9; i++) T.acc[a][i] = (f4){0.f, 0.f, 0.f, 0.f};
  }
  BAR();
  {
    auto nop = []() {};
    T.rd(smem, 1, T.P1, T.W1);
    WAITL0();
    BAR();
    SCHED();
    T.rd(smem + SLOT, 0, T.P0, T.W0);
    T.mm(T.P1, T.W1);
    auto sw = [&]() { asm volatile("v_mov_b32 %0, %2\n\tv_mov_b32 %1, %3" : "=&v"(T.wofs[0]), "=&v"(T.wofs[1]) : "v"(wW[0]), "v"(wW[1])); };
#pragma nounroll
    for (int r = 0; r < 5; r++) {
      T.template step<true>(smem, 1, nop);
      T.step(smem, 2, nop); T.step(smem, 3, nop);
      T.step(smem, 0, sw);
    }
    T.step(smem, 1, nop); T.step(smem, 2, nop);
  }
  char* own = smem;
  if (w != j) {
    const unsigned o = OFF_SLAB + ((tile * 4 + w) * 4 + j) * 9216 + lane * 16;
#pragma unroll
    for (int i = 0; i < 9; i++)
      ST16(((u4){pk2(T.acc[0][i][0], T.acc[0][i][1]), pk2(T.acc[0][i][2], T.acc[0][i][3]),
                 pk2(T.acc[1][i][0], T.acc[1][i][1]), pk2(T.acc[1][i][2], T.acc[1][i][3])}), rs, o + i * 1024);
  } else {
#pragma unroll
    for (int a = 0; a < 2; a++)
#pragma unroll
      for (int i = 0; i < 9; i++) *(f4*)(own + (a * 9 + i) * 1024 + lane * 16) = T.acc[a][i];
  }
  const unsigned inbox = __builtin_amdgcn_readfirstlane((L_IN + 2 * b + 1) * 128);
  WAITV(0);
  if (lane == 0) __hip_atomic_store(ctl(ws, L_IN + 2 * ((b & ~6) | (w << 1)) + 1) + j, 1u, RLX_AGENT);
  WAITL0();
  BAR();
  const int v = (w - j) & 3;
  f4 res[3][2];
#pragma unroll
  for (int r = 0; r < 3; r++) {
    const int i = (v + 4 * r < 9) ? v + 4 * r : v;
    const u2 dv = *(const u2*)(smem + LD_ST + 512 + i * 32 + g * 8);
#pragma unroll
    for (int a = 0; a < 2; a++) {
      const u2 xv = *(const u2*)(smem + LD_HT + (a * 16 + c) * 288 + i * 32 + g * 8);
      const f2 sv = *(const f2*)(smem + LD_ST + (a * 16 + c) * 8);
      const float bb = bflo(*(const u16*)(smem + LD_ST + 256 + (a * 16 + c) * 2));
      const f4 ow = *(const f4*)(own + (a * 9 + i) * 1024 + lane * 16);
#pragma unroll
      for (int e = 0; e < 4; e++) {
        const float xx = (e & 1) ? bfhi(xv[e >> 1]) : bflo(xv[e >> 1]);
        const float dd = (e & 1) ? bfhi(dv[e >> 1]) : bflo(dv[e >> 1]);
        res[r][a][e] = __builtin_fmaf((xx - sv[0]) * sv[1], dd, xx + bb) + ow[e];
      }
      asm volatile("" : "+v"(res[r][a]));
    }
  }
#pragma nounroll
  for (unsigned spins = 0; spins < SPIN_CAP; spins++) {
    u4 f;
    asm volatile("s_load_dwordx4 %0, %1, %2 glc\n\ts_waitcnt lgkmcnt(0)" : "=&s"(f) : "s"(ws), "s"(inbox) : "memory");
    if (f[0] & f[1] & f[2] & f[3]) break;
    __builtin_amdgcn_s_sleep(2);
  }
  u4 pv[3][3];
#pragma unroll
  for (int r = 0; r < 3; r++) {
    if (r < 2 || v == 0) {
#pragma unroll
      for (int k = 0; k < 3; k++) {
        const int src = (j + 1 + k) & 3;
        pv[r][k] = __builtin_amdgcn_raw_buffer_load_b128(rs, OFF_SLAB + ((tile * 4 + j) * 4 + src) * 9216 + (v + 4 * r) * 1024 + lane * 16, 0, 16);
      }
    }
  }
  const rsrc_t ro = __builtin_amdgcn_make_buffer_rsrc(A.out, 0, 2048 * D_ * 4, 0x00020000);
#pragma unroll
  for (int r = 0; r < 3; r++) {
    if (r < 2 || v == 0) {
#pragma unroll
      for (int a = 0; a < 2; a++) {
        const int s = st * 128 + j * 32 + a * 16 + c, d = dq * 144 + (v + 4 * r) * 16 + 4 * g;
        f4 o = res[r][a];
#pragma unroll
        for (int k = 0; k < 3; k++)
          o += (f4){bflo(pv[r][k][2 * a]), bfhi(pv[r][k][2 * a]), bflo(pv[r][k][2 * a + 1]), bfhi(pv[r][k][2 * a + 1])};
        __builtin_amdgcn_raw_buffer_store_b128(__builtin_bit_cast(u4, o), ro, (s * D_ + d) * 4, 0, 0);
      }
    }
  }
}

__device__ __forceinline__ void producer(const Args& A, char* smem, int lane, int pw) {
  char* ws = A.ws;
  const int b = blockIdx.x, xcd = b & 7, l = b >> 3;
  const bool tA = l < 24, tG1 = l < 27;
  const int t1 = xcd * 27 + l, ft = t1 / 6, mt = t1 % 6;
  const int r0 = lane >> 4, cc = lane & 15;
  const rsrc_t rs = __builtin_amdgcn_make_buffer_rsrc(ws, 0, WS_END, 0x00020000);
  const unsigned l16 = lane * 16;
  const rsrc_t rw = __builtin_amdgcn_make_buffer_rsrc((void*)A.w1, 0, 2048 * F_ * 2, 0x00020000);
  unsigned v1[4];
#pragma unroll
  for (int i = 0; i < 4; i++) { const int row = i * 4 + r0; v1[i] = row * (F_ * 2) + ((cc ^ ((row & 7) << 1)) << 4) - i * 1024; }
  auto is1w = [&](int k) {
    const unsigned wo = (k * 64 + pw * 32) * (F_ * 2) + ft * 256;
#pragma unroll
    for (int i = 0; i < 8; i++)
      blds4<0>(i & 3, rw, smem + (k & 3) * SLOT + WOFS + pw * 8192 + (i >> 2) * 4096, v1[i & 3], wo + (i >> 2) * 16 * (F_ * 2));
  };
  auto is1p = [&](int k) {
    const unsigned po = OFF_HF + (k * 72 + pw * 36 + mt * 6) * 1024;
#pragma unroll
    for (int i = 0; i < 6; i++) blds4<16>(i & 3, rs, smem + (k & 3) * SLOT + pw * 9216 + (i >> 2) * 4096, l16, po + (i >> 2) * 4096);
  };
  if (tA) {
    const int rc = (xcd >> 1) * 4 + (l & 3), cg = (xcd & 1) * 6 + (l >> 2);
    const int rr = lane >> 3;
    const rsrc_t rx = __builtin_amdgcn_make_buffer_rsrc((void*)A.x, 0, 2048 * D_ * 2, 0x00020000);
    unsigned xv[4]; const char* ba[2];
#pragma unroll
    for (int h = 0; h < 2; h++) {
      const int c16 = h * 8 + rr, sw = ((lane & 7) ^ (c16 >> 1)) << 4;
      xv[h] = ((c16 >> 2) * 8 + (c16 & 3)) * (D_ * 2) + sw - h * 1024;
      xv[h + 2] = xv[h] + 4 * (D_ * 2) - 2048;
      ba[h] = (const char*)((rr & 1) ? A.bim : A.bre) + (size_t)(cg * 48 + chan_of(c16 >> 1)) * (D_ * 2) + sw;
    }
    int kt = (l * 3 + (l >> 2)) % 9, s = 0;
    auto issue = [&]() {
      char* sl = smem + ((s + 3) & 3) * SLOT;
#pragma unroll
      for (int i = 0; i < 8; i++)
        blds4<0>(i & 3, rx, sl + pw * 8192 + (i >> 2) * 4096, xv[i & 3], (rc * 128 + pw * 64 + (i >> 2) * 32) * (D_ * 2) + kt * 128);
#pragma unroll
      for (int i = 0; i < 6; i++)
        glds16(ba[i & 1] + chan_of((pw * 3 + (i >> 1)) * 8) * (D_ * 2) + kt * 128, sl + 16384 + pw * 6144 + i * 1024);
      kt = kt == 8 ? 0 : kt + 1; s++;
    };
    BAR();
    issue(); issue();
    WAITV(14); BAR();
    issue();
    WAITV(14); BAR();
    issue(); issue();
#pragma nounroll
    for (int k = 2; k < 6; k++) { WAITV(28); BAR(); issue(); }
    WAITV(28); BAR();
    is1w(0);
    WAITV(22); BAR();
    if (pw == 0) blds<0, 16>(rs, smem + LD_ST, l16, OFF_STAT + ((xcd >> 1) * 4 + (l & 3)) * 1024);
    is1p(0); is1p(1); is1w(1);
    WAITV(28); BAR();
  } else {
    BAR();
    const char* base = (const char*)A.w1 + xcd * 1152;
    int pc = (l - 24) * 2 + pw;
    auto tw1 = [&]() { const int li = pc * 64 + lane; touch(base + (size_t)(li / 9) * (F_ * 2) + (li % 9) * 128, smem + LD_SCR); WAITV(3); };
    if (b != 255) { tw1(); pc += 16; tw1(); pc += 16; }
    lds_idle(sy(smem, Y_GO), 1u);
#pragma nounroll
    for (; pc < 16 * PF_B; pc += 16) tw1();
    if (!tG1) {
      pc = 16 * PF_B + (l - 27) * 2 + pw;
#pragma nounroll
      for (; pc < 288; pc += 10) tw1();
    }
    if (tG1) is1w(0);
    lds_wait(sy(smem, Y_HOK), 1u);
    if (tG1) { is1p(0); is1p(1); is1w(1); }
  }
  const int j = xcd >> 1, st = s_tile(A.w2, xcd, l >> 2), dq = l & 3;
  constexpr int ns = 5;
  const int rot = f_rot(A.w1, j);
  const rsrc_t rw2 = __builtin_amdgcn_make_buffer_rsrc((void*)A.w2, 0, F_ * 4096, 0x00020000);
  unsigned v2[4];
#pragma unroll
  for (int i = 0; i < 4; i++) { const int row = i * 4 + r0; v2[i] = row * 4096 + ((cc ^ ((row & 7) << 1)) << 4) - i * 1024; }
  auto is2p = [&](int k, int ks) {
    char* sl = smem + (k & 3) * SLOT + pw * 9216;
    const unsigned po = OFF_PF + (ks * 36 + dq * 9) * 1024;
#pragma unroll
    for (int i = 0; i < 9; i++) blds4<16>(i & 3, rs, sl + (i >> 2) * 4096, l16, po + (i >> 2) * 4096);
  };
  auto is2s = [&](int k) {
    char* sl = smem + (k & 3) * SLOT + WOFS + pw * 8192;
    const int ks = j * 9 + 2 * k - 1 + pw;
    const unsigned ho = OFF_HS + (st * 36 + ks) * 8192;
#pragma unroll
    for (int i = 0; i < 8; i++) blds4<16>(i & 3, rs, sl + (i >> 2) * 4096, l16, ho + (i >> 2) * 4096);
    is2p(k, 144 + ks);
  };
  auto wks = [&](int k) { return f_tile(j, rot, k >> 1) * 4 + (k & 1) * 2 + pw; };
  auto is2w = [&](int k) {
    char* sl = smem + ((ns + k) & 3) * SLOT + WOFS + pw * 8192;
    const unsigned wo = wks(k) * 32 * 4096 + st * 256;
#pragma unroll
    for (int i = 0; i < 8; i++) blds4<0>(i & 3, rw2, sl + (i >> 2) * 4096, v2[i & 3], wo + (i >> 2) * 16 * 4096);
  };
  if (tG1) {
    is1p(2); is1w(2);
    WAITV(28); BAR();
    is1p(3); is1w(3);
    WAITV(28); BAR();
    is1p(4); is1w(4);
#pragma nounroll
    for (int i = 5; i < 32; i++) { WAITV(28); BAR(); is1p(i); is1w(i); }
    WAITV(28); BAR();
    WAITV(14); BAR();
    WAITV(0);
    if (pw) is2s(0);
    BAR();
    is2s(1);
    BAR();
    __builtin_amdgcn_s_sleep(16);
    is2s(2);
  } else {
    int pc2 = (l - 27) * 2 + pw;
#pragma nounroll
    for (; pc2 < PF_W2 && __all(lds_get(sy(smem, Y_STOP)) == 0u); pc2 += 10) {
      touch(w2_row(A.w2, rot, pc2 * 4) + (lane >> 4) * 4096 + (lane & 15) * 128, smem + LD_SCR);
      WAITV(2);
    }
    lds_idle(sy(smem, Y_STOP), 1u);
    if (pw) is2s(0);
    is2s(1); is2s(2);
  }
  WAITV(17); BAR();
  BAR();
  is2s(3);
  WAITV(17); BAR();
  is2s(4);
  WAITV(17); BAR();
  is2w(0);
  if (tG1) touch(w2_row(A.w2, rot, SP0 - TL_ROWS + (l * 2 + pw) * 4) + (lane >> 4) * 4096 + (lane & 15) * 128, smem + LD_SCR);
  if (!tG1) { WAITV(8); BAR(); }
  if (pw == 0 || !tG1) swait6<3>(ctl(ws, L_IN + 2 * b));
  if (tG1) { WAITV(9); BAR(); }
  is2p(ns, wks(0));
  int k2 = 1;
  auto issue2 = [&]() { is2p(ns + k2, wks(k2)); is2w(k2); k2++; };
  issue2();
#pragma nounroll
  for (int i = 0; i < 16; i++) { WAITV(17); BAR(); issue2(); }
  WAITV(17); BAR();
  WAITV(0); BAR();
  BAR();
  WAITL0();
}

__device__ __forceinline__ void helper(const Args& A, char* smem, int lane) {
  char* ws = A.ws;
  const int b = blockIdx.x, xcd = b & 7, l = b >> 3;
  const bool tA = l < 24, tG1 = l < 27;
  const int rc = (xcd >> 1) * 4 + (l & 3), cg = (xcd & 1) * 6 + (l >> 2);
  const int L = lane < 48 ? lane : 47;
  const rsrc_t rs = __builtin_amdgcn_make_buffer_rsrc(ws, 0, WS_END, 0x00020000);
  const unsigned eo = OFF_E + (rc * 576 + cg * 48 + L) * 16;
  if (lane >= 54 || (tA && lane < 48))
    ST16(((u4){0u, 0u, 0u, 0u}), rs, lane == 63 ? (L_FL + b) * 128 : lane >= 54 ? (L_IN + 2 * b) * 128 + (lane - 54) * 16 : eo);
  if (lane < 16) *(unsigned*)(smem + LD_SY + lane * 4) = 0u;
  WAITL0();
  BAR();
  const char* hb = ws + (L_HB + (b & 63)) * 128;
  const unsigned h0 = sld(hb);
  bool moved = false;
  if (tA) {
    lds_wait(sy(smem, Y_LN), 4u);
    moved = sld(hb) != h0;
    BAR();
  }
#pragma nounroll
  for (unsigned spins = 0; spins < SPIN_CAP && !moved; spins++) {
    __builtin_amdgcn_s_sleep(2);
    moved = sld(hb) != h0;
  }
  if (lane == 0) __hip_atomic_fetch_add(ctl(ws, L_DN + (b & 31)), 1u, RLX_AGENT);
  WAITV(1);
  if (tA) BAR(); else lds_wait(sy(smem, Y_LN), 8u);
  if (lane < HREP) __hip_atomic_store(ctl(ws, L_HF + lane * 64 + (b >> 2)) + (b & 3), 1u, RLX_AGENT);
  auto see_rows = [&]() {
#pragma nounroll
    for (unsigned spins = 0; spins < SPIN_CAP; spins++) {
      const u4 v = __builtin_amdgcn_raw_buffer_load_b128(rs, (L_HF + (xcd * HREP / 8) * 64 + lane) * 128, 0, 16);
      if (__all((v[0] & v[1] & v[2] & v[3]) == 1u)) break;
      asm volatile("s_sleep 4" ::: "memory");
    }
    asm volatile("" ::: "memory");
  };
  if (tA) {
    BAR(); BAR(); BAR(); BAR();
    u4 hv;
    asm volatile("buffer_load_dwordx4 %0, %1, %2, 0 offen sc1\n\ts_barrier\n\ts_waitcnt vmcnt(0)"
                 : "=&v"(hv) : "v"((L_HF + (xcd * HREP / 8) * 64 + lane) * 128), "s"(rs) : "memory");
    if (!__all((hv[0] & hv[1] & hv[2] & hv[3]) == 1u)) see_rows();
    BAR(); BAR();
  }
  const gu32* sf = state_flag(ws, s_tile(A.w2, xcd, l >> 2), l & 3, lane);
  const int j = xcd >> 1, dq = l & 3;
  Spf sp = {A.w2, f_rot(A.w1, j), SP0 + 6 + (l >> 2), 12};
  int left = tA ? SP_H : 0;
  auto hbar = [&](int n) {
#pragma nounroll
    for (int k = 0; k < n; k++) { BAR(); if (left > 0) { sp.go(); left--; } }
  };
  auto lift = [&](int f) { if (lane == 0) __hip_atomic_store(flag(ws, b, f), 1u, RLX_AGENT); };
  auto finish = [&]() {
    hbar(2); BAR();
    if (tG1) {
      const int t1 = xcd * 27 + l, mt = t1 % 6, fi = t1 / 6 - 9 * j - f_rot(A.w1, j);
      const int q = (mt >= 3 ? 2 : 0) + ((lane >> 4) & 1), e = mt - (q * 3 >> 1);
      const int dst = ((lane & 7) * 4 + q) * 8 + 2 * j + ((lane >> 3) & 1);
      if (lane < 32 && (e == 0 || e == 1)) __hip_atomic_store(ctl(ws, L_IN + 2 * dst) + (fi < 0 ? fi + 9 : fi) * 2 + e, 1u, RLX_AGENT);
    }
    BAR();
    {
      const int s0 = s_tile(A.w2, xcd, l >> 2) * 128 + j * 32;
      const rsrc_t rx = __builtin_amdgcn_make_buffer_rsrc((void*)A.x, 0, 2048 * D_ * 2, 0x00020000);
#pragma nounroll
      for (int pc = 0; pc < 9; pc++) {
        const int ch = pc * 64 + lane;
        blds<0, 0>(rx, smem + LD_HT + pc * 1024, (ch / 18) * (D_ * 2) + (ch % 18) * 16, s0 * (D_ * 2) + dq * 288);
      }
      __builtin_amdgcn_raw_ptr_buffer_load_lds(rs, (LAS void*)(smem + LD_ST), 4, lane * 4, OFF_STAT + s0 * 8, 0, 16);
      touch((const char*)(A.b2 + s0) + (lane & 15) * 4, smem + LD_ST + 256);
      touch((const char*)(A.dskip + dq * 144) + lane * 4, smem + LD_ST + 512);
      touch((const char*)(A.dskip + dq * 144) + 256 + (lane & 7) * 4, smem + LD_ST + 768);
    }
    hbar(2);
    const gu32* f = ctl(ws, L_IN + 2 * b) + 6 + (lane < 12 ? lane : 0);
    unsigned fv;
    asm volatile("global_load_dword %0, %1, off sc1\n\ts_barrier\n\ts_barrier\n\ts_waitcnt vmcnt(0)" : "=&v"(fv) : "v"(f) : "memory");
    if (!__all(fv >= 1u)) wait_ge<2>(f, 1u);
    hbar(left > 1 ? left : 1);
    WAITL0();
  };
  auto see_states = [&]() {
    unsigned fv;
    asm volatile("global_load_dword %0, %1, off sc1\n\ts_barrier\n\ts_barrier\n\ts_waitcnt vmcnt(0)"
                 : "=&v"(fv) : "v"(sf) : "memory");
    if (!__all(fv >= 1u)) wait_ge<2>(sf, 1u);
    hbar(3);
  };
  if (!tA) {
    bump(smem, Y_GO, lane);
    if (tG1) __builtin_amdgcn_s_sleep(40); else __builtin_amdgcn_s_sleep(100);
    see_rows();
    lds_put(sy(smem, Y_HOK), 1u);
    if (tG1) {
#pragma nounroll
      for (int k = 0; k < 28; k++) BAR();
      see_states();
    } else {
      if (b < 252) {
        lds_idle(sy(smem, Y_SD), 4u);
        if (lane == 0) __hip_atomic_store(flag(ws, b, F_S), 1u, RLX_AGENT);
      }
      doze(flag(ws, blk_scan(s_tile(A.w2, xcd, l >> 2), 0), F_S));
      __builtin_amdgcn_s_sleep(32);
      wait_ge<8>(sf, 1u);
      lds_put(sy(smem, Y_STOP), 1u);
      WAITL0();
    }
    finish();
    return;
  }
  BAR();
  const f2 lm = *(const f2*)(smem + LD_LAM + L * 8);
  float pr = lm[0], pi = lm[1], r8 = 0.f, i8 = 0.f;
#pragma unroll
  for (int k = 0; k < 7; k++) {
    const float t = __builtin_fmaf(pr, pr, -(pi * pi)); pi = 2.f * pr * pi; pr = t;
    if (k == 2) { r8 = pr; i8 = pi; }
  }
  float r128 = pr, i128 = pi;
  asm volatile("" : "+v"(r8), "+v"(i8), "+v"(r128), "+v"(i128));
  hbar(2); BAR(); BAR(); BAR();
#define PIN2(a, b) asm volatile("s_barrier" : "+v"(a), "+v"(b) :: "memory")
  f2* agg = (f2*)(smem + LD_HT) + L;
  f2 ev[16];
#pragma unroll
  for (int k = 0; k < 16; k++) ev[k] = agg[k * 48];
  float xr = ev[0][0], xi = ev[0][1];
#pragma unroll
  for (int k = 1; k < 6; k++) cstep(xr, xi, r8, i8, ev[k][0], ev[k][1]);
  PIN2(xr, xi);
#pragma unroll
  for (int k = 6; k < 16; k++) cstep(xr, xi, r8, i8, ev[k][0], ev[k][1]);
  if (lane < 48) ST16(((u4){__float_as_uint(xr), 1u, __float_as_uint(xi), 1u}), rs, eo);
  hbar(3);
  u4 pv[15];
  auto ldp = [&](int k) { return __builtin_amdgcn_raw_buffer_load_b128(rs, k + rc >= 15 ? eo - (15 - k) * (576 * 16) : 0x80000000u, 0, 16); };
#pragma unroll
  for (int k = 0; k < 8; k++) pv[k] = ldp(k);
  hbar(1);
#pragma unroll
  for (int k = 8; k < 15; k++) pv[k] = ldp(k);
  unsigned z;
  asm volatile("s_barrier\n\ts_barrier\n\tv_mov_b32 %0, 0" : "=v"(z) :: "memory");
#pragma nounroll
  for (unsigned spins = 0; spins < SPIN_CAP; spins++) {
    unsigned cnt = z;
#pragma unroll
    for (int k = 0; k < 15; k++) cnt += pv[k][1] & pv[k][3];
    if (__all(cnt == (unsigned)rc)) break;
#pragma unroll
    for (int k = 0; k < 15; k++) pv[k] = ldp(k);
    asm volatile("" ::: "memory");
  }
  xr = 0.f; xi = 0.f;
  PIN2(xr, xi);
#pragma unroll
  for (int k = 0; k < 15; k++) cstep(xr, xi, r128, i128, __uint_as_float(pv[k][0]), __uint_as_float(pv[k][2]));
  PIN2(xr, xi);
#pragma unroll
  for (int k = 0; k < 16; k++) {
    const f2 e = ev[k];
    ev[k] = (f2){xr, xi};
    if (k < 15) cstep(xr, xi, r8, i8, e[0], e[1]);
  }
  PIN2(xr, xi);
  if (lane < 48) {
#pragma unroll
    for (int k = 0; k < 16; k++) agg[k * 48] = ev[k];
  }
  WAITL0();
  hbar(6); BAR();
  lift(F_S);
  hbar(5);
  see_states();
  finish();
}

__device__ __forceinline__ void extra(const Args& A, char* smem, int lane) {
  char* ws = A.ws;
  const int b = blockIdx.x, xcd = b & 7, l = b >> 3;
  const bool tA = l < 24;
  const int ch = tA ? ((xcd & 1) * 6 + (l >> 2)) * 48 + chan_of(lane < 48 ? lane : 47) : (((l + 9) * 8 + xcd) % 9) * 64 + lane;
  unsigned lrb = A.lre[ch], lib = A.lim[ch], dtb = A.logdt[ch];
  if (b == 255) {
    const rsrc_t rs = __builtin_amdgcn_make_buffer_rsrc(ws, 0, WS_END, 0x00020000);
#pragma unroll
    for (int i = 0; i <= HREP; i++) ST16(((u4){0u, 0u, 0u, 0u}), rs, (L_DN + i * 64 + lane) * 128);
  }
  BAR();
  if (b == 255) {
    WAITV(0);
    unsigned beat = 0;
#pragma nounroll
    for (unsigned spins = 0; spins < SPIN_CAP; spins++) {
      __hip_atomic_store(ctl(ws, L_HB + lane), ++beat, RLX_AGENT);
      if (spins < 2) { __builtin_amdgcn_s_sleep(10); continue; }
      const unsigned v = __hip_atomic_load(ctl(ws, L_DN + (lane & 31)), RLX_AGENT);
      if (__all(v >= 8u)) break;
    }
  }
  asm volatile("" : "+v"(lrb), "+v"(lib), "+v"(dtb));
  const float lr = bflo(lrb), li = bflo(lib);
  float dt = rb(expf(bflo(dtb)));
  float er = expf(dt * lr);
  if (tA) { asm volatile("s_barrier" : "+v"(dt), "+v"(er) :: "memory"); }
  float sn, cs;
  sincosf(dt * li, &sn, &cs);
  if (tA) { asm volatile("s_barrier" : "+v"(sn), "+v"(cs) :: "memory"); }
  const float ar = er * cs, ai = er * sn;
  const float nr = ar - 1.f, id = 2.f / (lr * lr + li * li);
  *(f2*)(smem + LD_LAM + lane * 8) = tA ? (f2){ar, ai} : (f2){(nr * lr + ai * li) * id, (ai * lr - nr * li) * id};
  if (!tA) {
    bump(smem, Y_PAR, lane);
    WAITL0();
    return;
  }
  WAITL0();
#pragma nounroll
  for (int k = 2; k < 9; k++) BAR();
  Spf sp = {A.w2, f_rot(A.w1, xcd >> 1), SP0 + (l >> 2), 12};
#pragma nounroll
  for (int i = 0; i < SP_X; i++) { BAR(); if (i == SP_H) sp.step = 6; sp.go(); }
  WAITL0();
}

constexpr int CODE_PIECES = 7;
__global__ __launch_bounds__(512, 2) void k_fused(Args A) {
  extern __shared__ __attribute__((aligned(16))) char smem[];
  const int lane = threadIdx.x & 63, w = __builtin_amdgcn_readfirstlane(threadIdx.x >> 6);
  const char* code;
  asm volatile("s_getpc_b64 %0" : "=s"(code));
  code = (const char*)((size_t)code & ~(size_t)255);
  if (w == 5 && blockIdx.x >= 192 && blockIdx.x < 192 + 8 * CODE_PIECES)
    touch(code + ((blockIdx.x - 192) >> 3) * 8192 + lane * 128, smem + LD_SCR);
  if (w < 4) consumer(A, smem, lane, w);
  else if (w < 6) producer(A, smem, lane, w - 4);
  else if (w == 6) helper(A, smem, lane);
  else extra(A, smem, lane);
}

extern "C" void kernel_launch(void* const* d_in, const int* in_sizes, int n_in, void* d_out, int out_size,
                              void* d_ws, size_t ws_size, hipStream_t stream) {
  Args a;
  a.x = (const u16*)d_in[0]; a.lre = (const u16*)d_in[1]; a.lim = (const u16*)d_in[2];
  a.bre = (const u16*)d_in[3]; a.bim = (const u16*)d_in[4]; a.cre = (const u16*)d_in[5]; a.cim = (const u16*)d_in[6];
  a.logdt = (const u16*)d_in[7]; a.dskip = (const u16*)d_in[8];
  a.w1 = (const u16*)d_in[10]; a.b1 = (const u16*)d_in[11]; a.w2 = (const u16*)d_in[12]; a.b2 = (const u16*)d_in[13];
  a.ws = (char*)d_ws; a.out = (float*)d_out;
  (void)hipFuncSetAttribute((const void*)k_fused, hipFuncAttributeMaxDynamicSharedMemorySize, LDS_BYTES);
  k_fused<<<256, 512, LDS_BYTES, stream>>>(a);
}
```
